```python
import torch

def _softmax(x, axis=-1):
    return torch.softmax(x, dim=axis)
def _gelu(x):
    return torch.nn.functional.gelu(x, approximate="tanh")
def _silu(x):
    return torch.nn.functional.silu(x)
def _sigmoid(x):
    return torch.sigmoid(x)
def _layernorm(x, axis=-1, eps=1e-5):
    m = x.mean(dim=axis, keepdim=True)
    v = x.var(dim=axis, keepdim=True, unbiased=False)
    return (x - m) / torch.sqrt(v + eps)
def _scatter_add(tgt, idx, src, axis=0):
    return tgt.clone().index_add_(axis, idx, src)
def _segment_sum(src, seg, n):
    out = torch.zeros((n,) + src.shape[1:], dtype=src.dtype)
    return out.index_add_(0, seg, src)
def _pad(x, pw):
    flat = tuple(v for pair in reversed(pw) for v in pair)
    return torch.nn.functional.pad(x, flat)

kernel_name = 'concat_heads__compressed_kv_cla__spectral_gfnet__prompt'

def setup_inputs():

    _w = (lambda a: (lambda t: t.to(torch.bfloat16) if t.is_floating_point() else t)(torch.as_tensor(a)))
    def _A_setup():
        B, H, Sq, Sk, D, M = 1, 32, 2048, 2048, 128, 576
        Dc = 4 * D
        return {
            "x": _w((lambda *s: torch.randn(*s, dtype=torch.bfloat16) * 3.2)(B, Sq, M)),
            "c_kv": _w((lambda *s: torch.randn(*s, dtype=torch.bfloat16) * 0.05)(B, Sk, Dc)),
            "w_q": _w((lambda *s: torch.randn(*s, dtype=torch.bfloat16) * 3.2)(M, H, D) * (M ** -0.5)),
            "w_uk": _w((lambda *s: torch.randn(*s, dtype=torch.bfloat16) * 3.2)(Dc, H, D) * (Dc ** -0.5)),
            "w_uv": _w((lambda *s: torch.randn(*s, dtype=torch.bfloat16) * 0.05)(Dc, H, D) * (Dc ** -0.5)),
            "w_o": _w((lambda *s: torch.randn(*s, dtype=torch.bfloat16) * 0.05)(H, D, M) * ((H * D) ** -0.5)),
        }
    _A_in = _A_setup()

    def _B_setup():
        B, S, D = 1, 2048, 576
        Df = D // 2 + 1
        return {
            "x": _w((lambda *s: torch.randn(*s, dtype=torch.bfloat16) * 0.05)(B, S, D)),
            "w_r": _w((lambda *s: torch.randn(*s, dtype=torch.bfloat16) * 0.05)(S, Df) * 0.02),
            "w_i": _w((lambda *s: torch.randn(*s, dtype=torch.bfloat16) * 0.05)(S, Df) * 0.02),
        }
    _B_in = _B_setup()

    return {
        "A_x": _A_in["x"],
        "A_c_kv": _A_in["c_kv"],
        "A_w_q": _A_in["w_q"],
        "A_w_uk": _A_in["w_uk"],
        "A_w_uv": _A_in["w_uv"],
        "A_w_o": _A_in["w_o"],
        "B_x": _B_in["x"],
        "B_w_r": _B_in["w_r"],
        "B_w_i": _B_in["w_i"],
    }

def reference(A_x, A_c_kv, A_w_q, A_w_uk, A_w_uv, A_w_o, B_x, B_w_r, B_w_i):
    def _A_ref(x, c_kv, w_q, w_uk, w_uv, w_o):
        B, Sq, M = x.shape[0], x.shape[1], x.shape[2]
        Sk = c_kv.shape[1]
        H, D = w_q.shape[1], w_q.shape[2]
        q = torch.einsum("bqm,mhd->bhqd", x, w_q)
        k = torch.einsum("bkc,chd->bhkd", c_kv, w_uk)
        v = torch.einsum("bkc,chd->bhkd", c_kv, w_uv)
        scores = torch.einsum("bhqd,bhkd->bhqk", q, k) * (D ** -0.5)
        off = Sk - Sq
        qi = torch.arange(Sq)[:, None]
        kj = torch.arange(Sk)[None, :]
        cmask = kj <= (qi + off)
        scores = torch.where(cmask[None, None], scores, -1e9)
        p = _softmax(scores, axis=-1)
        ctx = torch.einsum("bhqk,bhkd->bhqd", p, v)
        return torch.einsum("bhqd,hdm->bqm", ctx, w_o)

    def _B_ref(x, w_r, w_i):
        B, S, D = x.shape
        X = (lambda x, axes=(-2,-1): torch.fft.rfft2(x.float(), dim=axes))(x, axes=(-2, -1))
        W = w_r + 1j * w_i
        X = X * W
        y = (lambda x, s, axes=(-2,-1): torch.fft.irfft2(x, s=s, dim=axes).to(torch.bfloat16))(X, (S, D), axes=(-2, -1))
        return y

    a = _A_ref(A_x, A_c_kv, A_w_q, A_w_uk, A_w_uv, A_w_o)
    b = _B_ref(B_x, B_w_r, B_w_i)
    return torch.concatenate([a, b], axis=-1)
```

```cpp
#include <hip/hip_runtime.h>
#include <hip/hip_bf16.h>
#include <stdint.h>

__device__ const float g_tw576[1152] = {
1.0f,0.f,0.999940515f,-0.0109080914f,0.999761999f,-0.0218148846f,0.999464571f,-0.0327190831f,0.999048233f,-0.0436193869f,0.998512983f,-0.0545145012f,
0.997858942f,-0.0654031262f,0.997086108f,-0.0762839764f,0.99619472f,-0.0871557444f,0.99518472f,-0.0980171412f,0.994056344f,-0.108866878f,0.992809653f,-0.119703658f,
0.991444886f,-0.130526185f,0.989962101f,-0.141333193f,0.988361537f,-0.152123392f,0.986643314f,-0.162895471f,0.98480773f,-0.173648179f,0.982854962f,-0.184380218f,
0.980785251f,-0.195090324f,0.978598893f,-0.205777213f,0.976296008f,-0.21643962f,0.973876953f,-0.227076262f,0.971342087f,-0.237685889f,0.968691587f,-0.248267233f,
0.965925813f,-0.258819044f,0.96304512f,-0.269340068f,0.960049868f,-0.279829025f,0.956940353f,-0.290284663f,0.953716934f,-0.300705791f,0.950380087f,-0.311091155f,
0.94693011f,-0.321439475f,0.943367481f,-0.331749529f,0.939692616f,-0.342020154f,0.935905933f,-0.35225004f,0.932007849f,-0.362438053f,0.9279989f,-0.372582912f,
0.923879504f,-0.382683426f,0.919650197f,-0.392738432f,0.915311456f,-0.402746677f,0.910863817f,-0.412707031f,0.906307817f,-0.42261827f,0.901643932f,-0.432479203f,
0.896872759f,-0.442288697f,0.891994834f,-0.45204556f,0.887010813f,-0.4617486f,0.881921291f,-0.471396744f,0.876726747f,-0.480988771f,0.871427953f,-0.490523577f,
0.866025388f,-0.5f,0.860519826f,-0.509416938f,0.854911864f,-0.518773258f,0.849202156f,-0.528067827f,0.843391418f,-0.537299633f,0.837480366f,-0.546467423f,
0.831469595f,-0.555570245f,0.825359941f,-0.564606905f,0.819152057f,-0.57357645f,0.812846661f,-0.582477689f,0.806444585f,-0.591309667f,0.799946547f,-0.600071251f,
0.793353319f,-0.60876143f,0.786665738f,-0.617379189f,0.779884458f,-0.625923455f,0.773010433f,-0.634393275f,0.766044438f,-0.642787635f,0.758987308f,-0.651105464f,
0.751839817f,-0.659345806f,0.744602859f,-0.667507708f,0.737277329f,-0.675590217f,0.729864061f,-0.683592319f,0.722363949f,-0.691513062f,0.714777887f,-0.699351549f,
0.707106769f,-0.707106769f,0.699351549f,-0.714777887f,0.691513062f,-0.722363949f,0.683592319f,-0.729864061f,0.675590217f,-0.737277329f,0.667507708f,-0.744602859f,
0.659345806f,-0.751839817f,0.651105464f,-0.758987308f,0.642787635f,-0.766044438f,0.634393275f,-0.773010433f,0.625923455f,-0.779884458f,0.617379189f,-0.786665738f,
0.60876143f,-0.793353319f,0.600071251f,-0.799946547f,0.591309667f,-0.806444585f,0.582477689f,-0.812846661f,0.57357645f,-0.819152057f,0.564606905f,-0.825359941f,
0.555570245f,-0.831469595f,0.546467423f,-0.837480366f,0.537299633f,-0.843391418f,0.528067827f,-0.849202156f,0.518773258f,-0.854911864f,0.509416938f,-0.860519826f,
0.5f,-0.866025388f,0.490523577f,-0.871427953f,0.480988771f,-0.876726747f,0.471396744f,-0.881921291f,0.4617486f,-0.887010813f,0.45204556f,-0.891994834f,
0.442288697f,-0.896872759f,0.432479203f,-0.901643932f,0.42261827f,-0.906307817f,0.412707031f,-0.910863817f,0.402746677f,-0.915311456f,0.392738432f,-0.919650197f,
0.382683426f,-0.923879504f,0.372582912f,-0.9279989f,0.362438053f,-0.932007849f,0.35225004f,-0.935905933f,0.342020154f,-0.939692616f,0.331749529f,-0.943367481f,
0.321439475f,-0.94693011f,0.311091155f,-0.950380087f,0.300705791f,-0.953716934f,0.290284663f,-0.956940353f,0.279829025f,-0.960049868f,0.269340068f,-0.96304512f,
0.258819044f,-0.965925813f,0.248267233f,-0.968691587f,0.237685889f,-0.971342087f,0.227076262f,-0.973876953f,0.21643962f,-0.976296008f,0.205777213f,-0.978598893f,
0.195090324f,-0.980785251f,0.184380218f,-0.982854962f,0.173648179f,-0.98480773f,0.162895471f,-0.986643314f,0.152123392f,-0.988361537f,0.141333193f,-0.989962101f,
0.130526185f,-0.991444886f,0.119703658f,-0.992809653f,0.108866878f,-0.994056344f,0.0980171412f,-0.99518472f,0.0871557444f,-0.99619472f,0.0762839764f,-0.997086108f,
0.0654031262f,-0.997858942f,0.0545145012f,-0.998512983f,0.0436193869f,-0.999048233f,0.0327190831f,-0.999464571f,0.0218148846f,-0.999761999f,0.0109080914f,-0.999940515f,
0.f,-1.0f,-0.0109080914f,-0.999940515f,-0.0218148846f,-0.999761999f,-0.0327190831f,-0.999464571f,-0.0436193869f,-0.999048233f,-0.0545145012f,-0.998512983f,
-0.0654031262f,-0.997858942f,-0.0762839764f,-0.997086108f,-0.0871557444f,-0.99619472f,-0.0980171412f,-0.99518472f,-0.108866878f,-0.994056344f,-0.119703658f,-0.992809653f,
-0.130526185f,-0.991444886f,-0.141333193f,-0.989962101f,-0.152123392f,-0.988361537f,-0.162895471f,-0.986643314f,-0.173648179f,-0.98480773f,-0.184380218f,-0.982854962f,
-0.195090324f,-0.980785251f,-0.205777213f,-0.978598893f,-0.21643962f,-0.976296008f,-0.227076262f,-0.973876953f,-0.237685889f,-0.971342087f,-0.248267233f,-0.968691587f,
-0.258819044f,-0.965925813f,-0.269340068f,-0.96304512f,-0.279829025f,-0.960049868f,-0.290284663f,-0.956940353f,-0.300705791f,-0.953716934f,-0.311091155f,-0.950380087f,
-0.321439475f,-0.94693011f,-0.331749529f,-0.943367481f,-0.342020154f,-0.939692616f,-0.35225004f,-0.935905933f,-0.362438053f,-0.932007849f,-0.372582912f,-0.9279989f,
-0.382683426f,-0.923879504f,-0.392738432f,-0.919650197f,-0.402746677f,-0.915311456f,-0.412707031f,-0.910863817f,-0.42261827f,-0.906307817f,-0.432479203f,-0.901643932f,
-0.442288697f,-0.896872759f,-0.45204556f,-0.891994834f,-0.4617486f,-0.887010813f,-0.471396744f,-0.881921291f,-0.480988771f,-0.876726747f,-0.490523577f,-0.871427953f,
-0.5f,-0.866025388f,-0.509416938f,-0.860519826f,-0.518773258f,-0.854911864f,-0.528067827f,-0.849202156f,-0.537299633f,-0.843391418f,-0.546467423f,-0.837480366f,
-0.555570245f,-0.831469595f,-0.564606905f,-0.825359941f,-0.57357645f,-0.819152057f,-0.582477689f,-0.812846661f,-0.591309667f,-0.806444585f,-0.600071251f,-0.799946547f,
-0.60876143f,-0.793353319f,-0.617379189f,-0.786665738f,-0.625923455f,-0.779884458f,-0.634393275f,-0.773010433f,-0.642787635f,-0.766044438f,-0.651105464f,-0.758987308f,
-0.659345806f,-0.751839817f,-0.667507708f,-0.744602859f,-0.675590217f,-0.737277329f,-0.683592319f,-0.729864061f,-0.691513062f,-0.722363949f,-0.699351549f,-0.714777887f,
-0.707106769f,-0.707106769f,-0.714777887f,-0.699351549f,-0.722363949f,-0.691513062f,-0.729864061f,-0.683592319f,-0.737277329f,-0.675590217f,-0.744602859f,-0.667507708f,
-0.751839817f,-0.659345806f,-0.758987308f,-0.651105464f,-0.766044438f,-0.642787635f,-0.773010433f,-0.634393275f,-0.779884458f,-0.625923455f,-0.786665738f,-0.617379189f,
-0.793353319f,-0.60876143f,-0.799946547f,-0.600071251f,-0.806444585f,-0.591309667f,-0.812846661f,-0.582477689f,-0.819152057f,-0.57357645f,-0.825359941f,-0.564606905f,
-0.831469595f,-0.555570245f,-0.837480366f,-0.546467423f,-0.843391418f,-0.537299633f,-0.849202156f,-0.528067827f,-0.854911864f,-0.518773258f,-0.860519826f,-0.509416938f,
-0.866025388f,-0.5f,-0.871427953f,-0.490523577f,-0.876726747f,-0.480988771f,-0.881921291f,-0.471396744f,-0.887010813f,-0.4617486f,-0.891994834f,-0.45204556f,
-0.896872759f,-0.442288697f,-0.901643932f,-0.432479203f,-0.906307817f,-0.42261827f,-0.910863817f,-0.412707031f,-0.915311456f,-0.402746677f,-0.919650197f,-0.392738432f,
-0.923879504f,-0.382683426f,-0.9279989f,-0.372582912f,-0.932007849f,-0.362438053f,-0.935905933f,-0.35225004f,-0.939692616f,-0.342020154f,-0.943367481f,-0.331749529f,
-0.94693011f,-0.321439475f,-0.950380087f,-0.311091155f,-0.953716934f,-0.300705791f,-0.956940353f,-0.290284663f,-0.960049868f,-0.279829025f,-0.96304512f,-0.269340068f,
-0.965925813f,-0.258819044f,-0.968691587f,-0.248267233f,-0.971342087f,-0.237685889f,-0.973876953f,-0.227076262f,-0.976296008f,-0.21643962f,-0.978598893f,-0.205777213f,
-0.980785251f,-0.195090324f,-0.982854962f,-0.184380218f,-0.98480773f,-0.173648179f,-0.986643314f,-0.162895471f,-0.988361537f,-0.152123392f,-0.989962101f,-0.141333193f,
-0.991444886f,-0.130526185f,-0.992809653f,-0.119703658f,-0.994056344f,-0.108866878f,-0.99518472f,-0.0980171412f,-0.99619472f,-0.0871557444f,-0.997086108f,-0.0762839764f,
-0.997858942f,-0.0654031262f,-0.998512983f,-0.0545145012f,-0.999048233f,-0.0436193869f,-0.999464571f,-0.0327190831f,-0.999761999f,-0.0218148846f,-0.999940515f,-0.0109080914f,
-1.0f,0.f,-0.999940515f,0.0109080914f,-0.999761999f,0.0218148846f,-0.999464571f,0.0327190831f,-0.999048233f,0.0436193869f,-0.998512983f,0.0545145012f,
-0.997858942f,0.0654031262f,-0.997086108f,0.0762839764f,-0.99619472f,0.0871557444f,-0.99518472f,0.0980171412f,-0.994056344f,0.108866878f,-0.992809653f,0.119703658f,
-0.991444886f,0.130526185f,-0.989962101f,0.141333193f,-0.988361537f,0.152123392f,-0.986643314f,0.162895471f,-0.98480773f,0.173648179f,-0.982854962f,0.184380218f,
-0.980785251f,0.195090324f,-0.978598893f,0.205777213f,-0.976296008f,0.21643962f,-0.973876953f,0.227076262f,-0.971342087f,0.237685889f,-0.968691587f,0.248267233f,
-0.965925813f,0.258819044f,-0.96304512f,0.269340068f,-0.960049868f,0.279829025f,-0.956940353f,0.290284663f,-0.953716934f,0.300705791f,-0.950380087f,0.311091155f,
-0.94693011f,0.321439475f,-0.943367481f,0.331749529f,-0.939692616f,0.342020154f,-0.935905933f,0.35225004f,-0.932007849f,0.362438053f,-0.9279989f,0.372582912f,
-0.923879504f,0.382683426f,-0.919650197f,0.392738432f,-0.915311456f,0.402746677f,-0.910863817f,0.412707031f,-0.906307817f,0.42261827f,-0.901643932f,0.432479203f,
-0.896872759f,0.442288697f,-0.891994834f,0.45204556f,-0.887010813f,0.4617486f,-0.881921291f,0.471396744f,-0.876726747f,0.480988771f,-0.871427953f,0.490523577f,
-0.866025388f,0.5f,-0.860519826f,0.509416938f,-0.854911864f,0.518773258f,-0.849202156f,0.528067827f,-0.843391418f,0.537299633f,-0.837480366f,0.546467423f,
-0.831469595f,0.555570245f,-0.825359941f,0.564606905f,-0.819152057f,0.57357645f,-0.812846661f,0.582477689f,-0.806444585f,0.591309667f,-0.799946547f,0.600071251f,
-0.793353319f,0.60876143f,-0.786665738f,0.617379189f,-0.779884458f,0.625923455f,-0.773010433f,0.634393275f,-0.766044438f,0.642787635f,-0.758987308f,0.651105464f,
-0.751839817f,0.659345806f,-0.744602859f,0.667507708f,-0.737277329f,0.675590217f,-0.729864061f,0.683592319f,-0.722363949f,0.691513062f,-0.714777887f,0.699351549f,
-0.707106769f,0.707106769f,-0.699351549f,0.714777887f,-0.691513062f,0.722363949f,-0.683592319f,0.729864061f,-0.675590217f,0.737277329f,-0.667507708f,0.744602859f,
-0.659345806f,0.751839817f,-0.651105464f,0.758987308f,-0.642787635f,0.766044438f,-0.634393275f,0.773010433f,-0.625923455f,0.779884458f,-0.617379189f,0.786665738f,
-0.60876143f,0.793353319f,-0.600071251f,0.799946547f,-0.591309667f,0.806444585f,-0.582477689f,0.812846661f,-0.57357645f,0.819152057f,-0.564606905f,0.825359941f,
-0.555570245f,0.831469595f,-0.546467423f,0.837480366f,-0.537299633f,0.843391418f,-0.528067827f,0.849202156f,-0.518773258f,0.854911864f,-0.509416938f,0.860519826f,
-0.5f,0.866025388f,-0.490523577f,0.871427953f,-0.480988771f,0.876726747f,-0.471396744f,0.881921291f,-0.4617486f,0.887010813f,-0.45204556f,0.891994834f,
-0.442288697f,0.896872759f,-0.432479203f,0.901643932f,-0.42261827f,0.906307817f,-0.412707031f,0.910863817f,-0.402746677f,0.915311456f,-0.392738432f,0.919650197f,
-0.382683426f,0.923879504f,-0.372582912f,0.9279989f,-0.362438053f,0.932007849f,-0.35225004f,0.935905933f,-0.342020154f,0.939692616f,-0.331749529f,0.943367481f,
-0.321439475f,0.94693011f,-0.311091155f,0.950380087f,-0.300705791f,0.953716934f,-0.290284663f,0.956940353f,-0.279829025f,0.960049868f,-0.269340068f,0.96304512f,
-0.258819044f,0.965925813f,-0.248267233f,0.968691587f,-0.237685889f,0.971342087f,-0.227076262f,0.973876953f,-0.21643962f,0.976296008f,-0.205777213f,0.978598893f,
-0.195090324f,0.980785251f,-0.184380218f,0.982854962f,-0.173648179f,0.98480773f,-0.162895471f,0.986643314f,-0.152123392f,0.988361537f,-0.141333193f,0.989962101f,
-0.130526185f,0.991444886f,-0.119703658f,0.992809653f,-0.108866878f,0.994056344f,-0.0980171412f,0.99518472f,-0.0871557444f,0.99619472f,-0.0762839764f,0.997086108f,
-0.0654031262f,0.997858942f,-0.0545145012f,0.998512983f,-0.0436193869f,0.999048233f,-0.0327190831f,0.999464571f,-0.0218148846f,0.999761999f,-0.0109080914f,0.999940515f,
0.f,1.0f,0.0109080914f,0.999940515f,0.0218148846f,0.999761999f,0.0327190831f,0.999464571f,0.0436193869f,0.999048233f,0.0545145012f,0.998512983f,
0.0654031262f,0.997858942f,0.0762839764f,0.997086108f,0.0871557444f,0.99619472f,0.0980171412f,0.99518472f,0.108866878f,0.994056344f,0.119703658f,0.992809653f,
0.130526185f,0.991444886f,0.141333193f,0.989962101f,0.152123392f,0.988361537f,0.162895471f,0.986643314f,0.173648179f,0.98480773f,0.184380218f,0.982854962f,
0.195090324f,0.980785251f,0.205777213f,0.978598893f,0.21643962f,0.976296008f,0.227076262f,0.973876953f,0.237685889f,0.971342087f,0.248267233f,0.968691587f,
0.258819044f,0.965925813f,0.269340068f,0.96304512f,0.279829025f,0.960049868f,0.290284663f,0.956940353f,0.300705791f,0.953716934f,0.311091155f,0.950380087f,
0.321439475f,0.94693011f,0.331749529f,0.943367481f,0.342020154f,0.939692616f,0.35225004f,0.935905933f,0.362438053f,0.932007849f,0.372582912f,0.9279989f,
0.382683426f,0.923879504f,0.392738432f,0.919650197f,0.402746677f,0.915311456f,0.412707031f,0.910863817f,0.42261827f,0.906307817f,0.432479203f,0.901643932f,
0.442288697f,0.896872759f,0.45204556f,0.891994834f,0.4617486f,0.887010813f,0.471396744f,0.881921291f,0.480988771f,0.876726747f,0.490523577f,0.871427953f,
0.5f,0.866025388f,0.509416938f,0.860519826f,0.518773258f,0.854911864f,0.528067827f,0.849202156f,0.537299633f,0.843391418f,0.546467423f,0.837480366f,
0.555570245f,0.831469595f,0.564606905f,0.825359941f,0.57357645f,0.819152057f,0.582477689f,0.812846661f,0.591309667f,0.806444585f,0.600071251f,0.799946547f,
0.60876143f,0.793353319f,0.617379189f,0.786665738f,0.625923455f,0.779884458f,0.634393275f,0.773010433f,0.642787635f,0.766044438f,0.651105464f,0.758987308f,
0.659345806f,0.751839817f,0.667507708f,0.744602859f,0.675590217f,0.737277329f,0.683592319f,0.729864061f,0.691513062f,0.722363949f,0.699351549f,0.714777887f,
0.707106769f,0.707106769f,0.714777887f,0.699351549f,0.722363949f,0.691513062f,0.729864061f,0.683592319f,0.737277329f,0.675590217f,0.744602859f,0.667507708f,
0.751839817f,0.659345806f,0.758987308f,0.651105464f,0.766044438f,0.642787635f,0.773010433f,0.634393275f,0.779884458f,0.625923455f,0.786665738f,0.617379189f,
0.793353319f,0.60876143f,0.799946547f,0.600071251f,0.806444585f,0.591309667f,0.812846661f,0.582477689f,0.819152057f,0.57357645f,0.825359941f,0.564606905f,
0.831469595f,0.555570245f,0.837480366f,0.546467423f,0.843391418f,0.537299633f,0.849202156f,0.528067827f,0.854911864f,0.518773258f,0.860519826f,0.509416938f,
0.866025388f,0.5f,0.871427953f,0.490523577f,0.876726747f,0.480988771f,0.881921291f,0.471396744f,0.887010813f,0.4617486f,0.891994834f,0.45204556f,
0.896872759f,0.442288697f,0.901643932f,0.432479203f,0.906307817f,0.42261827f,0.910863817f,0.412707031f,0.915311456f,0.402746677f,0.919650197f,0.392738432f,
0.923879504f,0.382683426f,0.9279989f,0.372582912f,0.932007849f,0.362438053f,0.935905933f,0.35225004f,0.939692616f,0.342020154f,0.943367481f,0.331749529f,
0.94693011f,0.321439475f,0.950380087f,0.311091155f,0.953716934f,0.300705791f,0.956940353f,0.290284663f,0.960049868f,0.279829025f,0.96304512f,0.269340068f,
0.965925813f,0.258819044f,0.968691587f,0.248267233f,0.971342087f,0.237685889f,0.973876953f,0.227076262f,0.976296008f,0.21643962f,0.978598893f,0.205777213f,
0.980785251f,0.195090324f,0.982854962f,0.184380218f,0.98480773f,0.173648179f,0.986643314f,0.162895471f,0.988361537f,0.152123392f,0.989962101f,0.141333193f,
0.991444886f,0.130526185f,0.992809653f,0.119703658f,0.994056344f,0.108866878f,0.99518472f,0.0980171412f,0.99619472f,0.0871557444f,0.997086108f,0.0762839764f,
0.997858942f,0.0654031262f,0.998512983f,0.0545145012f,0.999048233f,0.0436193869f,0.999464571f,0.0327190831f,0.999761999f,0.0218148846f,0.999940515f,0.0109080914f,
};
__device__ const float g_tw2048[4096] = {
1.0f,0.f,0.999995291f,-0.00306795677f,0.999981165f,-0.00613588467f,0.999957621f,-0.00920375437f,0.999924719f,-0.0122715384f,0.99988234f,-0.015339206f,
0.999830604f,-0.0184067301f,0.99976939f,-0.0214740802f,0.999698818f,-0.024541229f,0.999618828f,-0.027608145f,0.999529421f,-0.030674804f,0.999430597f,-0.0337411724f,
0.999322355f,-0.0368072242f,0.999204755f,-0.0398729257f,0.999077737f,-0.0429382585f,0.998941302f,-0.0460031815f,0.99879545f,-0.0490676761f,0.998640239f,-0.052131705f,
0.998475552f,-0.0551952459f,0.998301566f,-0.0582582653f,0.998118103f,-0.061320737f,0.997925282f,-0.0643826276f,0.997723043f,-0.0674439222f,0.997511446f,-0.070504576f,
0.997290432f,-0.0735645667f,0.997060061f,-0.0766238645f,0.996820271f,-0.0796824396f,0.996571124f,-0.0827402622f,0.996312618f,-0.0857973099f,0.996044695f,-0.0888535529f,
0.995767415f,-0.0919089541f,0.995480776f,-0.0949634984f,0.99518472f,-0.0980171412f,0.994879305f,-0.10106986f,0.994564593f,-0.104121633f,0.994240463f,-0.107172422f,
0.993906975f,-0.110222206f,0.993564129f,-0.113270953f,0.993211925f,-0.116318628f,0.992850423f,-0.119365215f,0.992479563f,-0.122410677f,0.992099285f,-0.125454977f,
0.991709769f,-0.128498107f,0.991310835f,-0.13154003f,0.990902662f,-0.134580702f,0.990485072f,-0.137620121f,0.990058184f,-0.140658244f,0.989621997f,-0.143695027f,
0.989176512f,-0.146730468f,0.988721669f,-0.149764538f,0.988257587f,-0.152797192f,0.987784147f,-0.155828401f,0.987301409f,-0.15885815f,0.986809373f,-0.161886394f,
0.986308098f,-0.164913118f,0.985797524f,-0.167938292f,0.985277653f,-0.170961887f,0.984748483f,-0.173983872f,0.984210074f,-0.177004218f,0.983662426f,-0.180022895f,
0.983105481f,-0.183039889f,0.982539296f,-0.186055154f,0.981963873f,-0.18906866f,0.981379211f,-0.192080393f,0.980785251f,-0.195090324f,0.980182111f,-0.198098406f,
0.979569793f,-0.201104641f,0.978948176f,-0.204108968f,0.97831738f,-0.207111374f,0.977677345f,-0.210111842f,0.977028131f,-0.213110313f,0.976369739f,-0.216106802f,
0.975702107f,-0.219101235f,0.975025356f,-0.222093627f,0.974339366f,-0.225083917f,0.973644257f,-0.228072077f,0.972939968f,-0.231058106f,0.972226501f,-0.234041959f,
0.971503913f,-0.237023607f,0.970772147f,-0.24000302f,0.970031261f,-0.242980182f,0.969281256f,-0.24595505f,0.968522072f,-0.248927608f,0.967753828f,-0.251897812f,
0.966976464f,-0.254865646f,0.966189981f,-0.257831097f,0.965394437f,-0.260794103f,0.964589775f,-0.263754666f,0.963776052f,-0.266712755f,0.962953269f,-0.269668311f,
0.962121427f,-0.272621363f,0.961280465f,-0.275571823f,0.960430503f,-0.27851969f,0.95957154f,-0.281464934f,0.958703458f,-0.284407526f,0.957826436f,-0.287347466f,
0.956940353f,-0.290284663f,0.95604527f,-0.293219149f,0.955141187f,-0.296150893f,0.954228103f,-0.299079835f,0.953306019f,-0.302005947f,0.952374995f,-0.304929227f,
0.95143503f,-0.307849646f,0.950486064f,-0.310767144f,0.949528158f,-0.313681751f,0.94856137f,-0.316593379f,0.947585583f,-0.319502026f,0.946600914f,-0.322407693f,
0.945607305f,-0.32531029f,0.944604814f,-0.328209847f,0.943593442f,-0.331106305f,0.94257319f,-0.333999664f,0.941544056f,-0.336889863f,0.940506041f,-0.339776874f,
0.939459205f,-0.342660725f,0.938403547f,-0.345541328f,0.937339008f,-0.348418683f,0.936265647f,-0.351292759f,0.935183525f,-0.354163527f,0.934092522f,-0.357030958f,
0.932992816f,-0.359895051f,0.931884289f,-0.362755716f,0.93076694f,-0.365612984f,0.929640889f,-0.368466824f,0.928506076f,-0.371317208f,0.927362502f,-0.374164075f,
0.926210225f,-0.377007425f,0.925049245f,-0.379847199f,0.923879504f,-0.382683426f,0.92270112f,-0.385516047f,0.921514034f,-0.388345033f,0.920318305f,-0.391170382f,
0.919113874f,-0.393992037f,0.917900801f,-0.396809995f,0.916679084f,-0.399624199f,0.915448725f,-0.402434647f,0.914209783f,-0.405241311f,0.912962198f,-0.408044159f,
0.91170603f,-0.410843164f,0.910441279f,-0.413638324f,0.909168005f,-0.416429549f,0.907886088f,-0.419216901f,0.906595707f,-0.422000259f,0.905296743f,-0.424779683f,
0.903989315f,-0.427555084f,0.902673304f,-0.430326492f,0.901348829f,-0.433093816f,0.900015891f,-0.435857087f,0.898674488f,-0.438616246f,0.897324562f,-0.441371262f,
0.895966232f,-0.444122136f,0.894599497f,-0.446868837f,0.893224299f,-0.449611336f,0.891840696f,-0.452349573f,0.890448749f,-0.455083579f,0.889048338f,-0.457813293f,
0.887639642f,-0.460538715f,0.886222541f,-0.463259786f,0.884797096f,-0.465976506f,0.883363366f,-0.468688816f,0.881921291f,-0.471396744f,0.880470872f,-0.474100202f,
0.879012227f,-0.47679922f,0.877545297f,-0.479493767f,0.876070082f,-0.482183784f,0.874586642f,-0.484869242f,0.873094976f,-0.487550169f,0.871595085f,-0.490226477f,
0.870086968f,-0.492898196f,0.868570685f,-0.495565265f,0.867046237f,-0.498227656f,0.865513623f,-0.500885367f,0.863972843f,-0.50353837f,0.862423956f,-0.506186664f,
0.860866964f,-0.50883013f,0.859301805f,-0.511468828f,0.857728601f,-0.514102757f,0.856147349f,-0.516731799f,0.854557991f,-0.519356012f,0.852960587f,-0.521975279f,
0.851355195f,-0.524589658f,0.849741757f,-0.527199149f,0.848120332f,-0.529803634f,0.84649092f,-0.532403111f,0.84485358f,-0.534997642f,0.843208253f,-0.537587047f,
0.841554999f,-0.540171444f,0.839893818f,-0.542750776f,0.838224709f,-0.545324981f,0.836547732f,-0.547894061f,0.834862888f,-0.550457954f,0.833170176f,-0.553016722f,
0.831469595f,-0.555570245f,0.829761207f,-0.558118522f,0.82804507f,-0.560661554f,0.826321065f,-0.563199341f,0.824589312f,-0.565731823f,0.82284981f,-0.568258941f,
0.8211025f,-0.570780754f,0.819347501f,-0.573297143f,0.817584813f,-0.575808167f,0.815814435f,-0.578313768f,0.81403631f,-0.580813944f,0.812250614f,-0.583308637f,
0.81045717f,-0.585797846f,0.808656156f,-0.588281572f,0.806847572f,-0.590759695f,0.805031359f,-0.593232274f,0.803207517f,-0.59569931f,0.801376164f,-0.598160684f,
0.799537241f,-0.600616455f,0.797690868f,-0.603066623f,0.795836926f,-0.605511069f,0.793975472f,-0.607949793f,0.792106569f,-0.610382795f,0.790230215f,-0.612810075f,
0.78834641f,-0.615231574f,0.786455214f,-0.61764729f,0.784556568f,-0.620057225f,0.78265059f,-0.622461259f,0.780737221f,-0.624859512f,0.778816521f,-0.627251804f,
0.77688849f,-0.629638255f,0.774953127f,-0.632018745f,0.773010433f,-0.634393275f,0.771060526f,-0.636761844f,0.769103348f,-0.639124453f,0.767138898f,-0.641481042f,
0.765167236f,-0.643831551f,0.763188422f,-0.64617604f,0.761202395f,-0.64851439f,0.759209216f,-0.65084666f,0.757208824f,-0.653172851f,0.755201399f,-0.655492842f,
0.753186822f,-0.657806695f,0.751165152f,-0.660114348f,0.749136388f,-0.662415802f,0.747100592f,-0.664710999f,0.745057762f,-0.666999936f,0.743007958f,-0.669282615f,
0.740951121f,-0.671558976f,0.73888731f,-0.673829019f,0.736816585f,-0.676092684f,0.734738886f,-0.678350031f,0.732654274f,-0.680601001f,0.730562747f,-0.682845533f,
0.728464365f,-0.685083687f,0.726359129f,-0.687315345f,0.724247098f,-0.689540565f,0.722128212f,-0.691759229f,0.720002532f,-0.693971455f,0.717870057f,-0.696177125f,
0.715730846f,-0.698376238f,0.71358484f,-0.700568795f,0.711432219f,-0.702754736f,0.709272802f,-0.704934061f,0.707106769f,-0.707106769f,0.704934061f,-0.709272802f,
0.702754736f,-0.711432219f,0.700568795f,-0.71358484f,0.698376238f,-0.715730846f,0.696177125f,-0.717870057f,0.693971455f,-0.720002532f,0.691759229f,-0.722128212f,
0.689540565f,-0.724247098f,0.687315345f,-0.726359129f,0.685083687f,-0.728464365f,0.682845533f,-0.730562747f,0.680601001f,-0.732654274f,0.678350031f,-0.734738886f,
0.676092684f,-0.736816585f,0.673829019f,-0.73888731f,0.671558976f,-0.740951121f,0.669282615f,-0.743007958f,0.666999936f,-0.745057762f,0.664710999f,-0.747100592f,
0.662415802f,-0.749136388f,0.660114348f,-0.751165152f,0.657806695f,-0.753186822f,0.655492842f,-0.755201399f,0.653172851f,-0.757208824f,0.65084666f,-0.759209216f,
0.64851439f,-0.761202395f,0.64617604f,-0.763188422f,0.643831551f,-0.765167236f,0.641481042f,-0.767138898f,0.639124453f,-0.769103348f,0.636761844f,-0.771060526f,
0.634393275f,-0.773010433f,0.632018745f,-0.774953127f,0.629638255f,-0.77688849f,0.627251804f,-0.778816521f,0.624859512f,-0.780737221f,0.622461259f,-0.78265059f,
0.620057225f,-0.784556568f,0.61764729f,-0.786455214f,0.615231574f,-0.78834641f,0.612810075f,-0.790230215f,0.610382795f,-0.792106569f,0.607949793f,-0.793975472f,
0.605511069f,-0.795836926f,0.603066623f,-0.797690868f,0.600616455f,-0.799537241f,0.598160684f,-0.801376164f,0.59569931f,-0.803207517f,0.593232274f,-0.805031359f,
0.590759695f,-0.806847572f,0.588281572f,-0.808656156f,0.585797846f,-0.81045717f,0.583308637f,-0.812250614f,0.580813944f,-0.81403631f,0.578313768f,-0.815814435f,
0.575808167f,-0.817584813f,0.573297143f,-0.819347501f,0.570780754f,-0.8211025f,0.568258941f,-0.82284981f,0.565731823f,-0.824589312f,0.563199341f,-0.826321065f,
0.560661554f,-0.82804507f,0.558118522f,-0.829761207f,0.555570245f,-0.831469595f,0.553016722f,-0.833170176f,0.550457954f,-0.834862888f,0.547894061f,-0.836547732f,
0.545324981f,-0.838224709f,0.542750776f,-0.839893818f,0.540171444f,-0.841554999f,0.537587047f,-0.843208253f,0.534997642f,-0.84485358f,0.532403111f,-0.84649092f,
0.529803634f,-0.848120332f,0.527199149f,-0.849741757f,0.524589658f,-0.851355195f,0.521975279f,-0.852960587f,0.519356012f,-0.854557991f,0.516731799f,-0.856147349f,
0.514102757f,-0.857728601f,0.511468828f,-0.859301805f,0.50883013f,-0.860866964f,0.506186664f,-0.862423956f,0.50353837f,-0.863972843f,0.500885367f,-0.865513623f,
0.498227656f,-0.867046237f,0.495565265f,-0.868570685f,0.492898196f,-0.870086968f,0.490226477f,-0.871595085f,0.487550169f,-0.873094976f,0.484869242f,-0.874586642f,
0.482183784f,-0.876070082f,0.479493767f,-0.877545297f,0.47679922f,-0.879012227f,0.474100202f,-0.880470872f,0.471396744f,-0.881921291f,0.468688816f,-0.883363366f,
0.465976506f,-0.884797096f,0.463259786f,-0.886222541f,0.460538715f,-0.887639642f,0.457813293f,-0.889048338f,0.455083579f,-0.890448749f,0.452349573f,-0.891840696f,
0.449611336f,-0.893224299f,0.446868837f,-0.894599497f,0.444122136f,-0.895966232f,0.441371262f,-0.897324562f,0.438616246f,-0.898674488f,0.435857087f,-0.900015891f,
0.433093816f,-0.901348829f,0.430326492f,-0.902673304f,0.427555084f,-0.903989315f,0.424779683f,-0.905296743f,0.422000259f,-0.906595707f,0.419216901f,-0.907886088f,
0.416429549f,-0.909168005f,0.413638324f,-0.910441279f,0.410843164f,-0.91170603f,0.408044159f,-0.912962198f,0.405241311f,-0.914209783f,0.402434647f,-0.915448725f,
0.399624199f,-0.916679084f,0.396809995f,-0.917900801f,0.393992037f,-0.919113874f,0.391170382f,-0.920318305f,0.388345033f,-0.921514034f,0.385516047f,-0.92270112f,
0.382683426f,-0.923879504f,0.379847199f,-0.925049245f,0.377007425f,-0.926210225f,0.374164075f,-0.927362502f,0.371317208f,-0.928506076f,0.368466824f,-0.929640889f,
0.365612984f,-0.93076694f,0.362755716f,-0.931884289f,0.359895051f,-0.932992816f,0.357030958f,-0.934092522f,0.354163527f,-0.935183525f,0.351292759f,-0.936265647f,
0.348418683f,-0.937339008f,0.345541328f,-0.938403547f,0.342660725f,-0.939459205f,0.339776874f,-0.940506041f,0.336889863f,-0.941544056f,0.333999664f,-0.94257319f,
0.331106305f,-0.943593442f,0.328209847f,-0.944604814f,0.32531029f,-0.945607305f,0.322407693f,-0.946600914f,0.319502026f,-0.947585583f,0.316593379f,-0.94856137f,
0.313681751f,-0.949528158f,0.310767144f,-0.950486064f,0.307849646f,-0.95143503f,0.304929227f,-0.952374995f,0.302005947f,-0.953306019f,0.299079835f,-0.954228103f,
0.296150893f,-0.955141187f,0.293219149f,-0.95604527f,0.290284663f,-0.956940353f,0.287347466f,-0.957826436f,0.284407526f,-0.958703458f,0.281464934f,-0.95957154f,
0.27851969f,-0.960430503f,0.275571823f,-0.961280465f,0.272621363f,-0.962121427f,0.269668311f,-0.962953269f,0.266712755f,-0.963776052f,0.263754666f,-0.964589775f,
0.260794103f,-0.965394437f,0.257831097f,-0.966189981f,0.254865646f,-0.966976464f,0.251897812f,-0.967753828f,0.248927608f,-0.968522072f,0.24595505f,-0.969281256f,
0.242980182f,-0.970031261f,0.24000302f,-0.970772147f,0.237023607f,-0.971503913f,0.234041959f,-0.972226501f,0.231058106f,-0.972939968f,0.228072077f,-0.973644257f,
0.225083917f,-0.974339366f,0.222093627f,-0.975025356f,0.219101235f,-0.975702107f,0.216106802f,-0.976369739f,0.213110313f,-0.977028131f,0.210111842f,-0.977677345f,
0.207111374f,-0.97831738f,0.204108968f,-0.978948176f,0.201104641f,-0.979569793f,0.198098406f,-0.980182111f,0.195090324f,-0.980785251f,0.192080393f,-0.981379211f,
0.18906866f,-0.981963873f,0.186055154f,-0.982539296f,0.183039889f,-0.983105481f,0.180022895f,-0.983662426f,0.177004218f,-0.984210074f,0.173983872f,-0.984748483f,
0.170961887f,-0.985277653f,0.167938292f,-0.985797524f,0.164913118f,-0.986308098f,0.161886394f,-0.986809373f,0.15885815f,-0.987301409f,0.155828401f,-0.987784147f,
0.152797192f,-0.988257587f,0.149764538f,-0.988721669f,0.146730468f,-0.989176512f,0.143695027f,-0.989621997f,0.140658244f,-0.990058184f,0.137620121f,-0.990485072f,
0.134580702f,-0.990902662f,0.13154003f,-0.991310835f,0.128498107f,-0.991709769f,0.125454977f,-0.992099285f,0.122410677f,-0.992479563f,0.119365215f,-0.992850423f,
0.116318628f,-0.993211925f,0.113270953f,-0.993564129f,0.110222206f,-0.993906975f,0.107172422f,-0.994240463f,0.104121633f,-0.994564593f,0.10106986f,-0.994879305f,
0.0980171412f,-0.99518472f,0.0949634984f,-0.995480776f,0.0919089541f,-0.995767415f,0.0888535529f,-0.996044695f,0.0857973099f,-0.996312618f,0.0827402622f,-0.996571124f,
0.0796824396f,-0.996820271f,0.0766238645f,-0.997060061f,0.0735645667f,-0.997290432f,0.070504576f,-0.997511446f,0.0674439222f,-0.997723043f,0.0643826276f,-0.997925282f,
0.061320737f,-0.998118103f,0.0582582653f,-0.998301566f,0.0551952459f,-0.998475552f,0.052131705f,-0.998640239f,0.0490676761f,-0.99879545f,0.0460031815f,-0.998941302f,
0.0429382585f,-0.999077737f,0.0398729257f,-0.999204755f,0.0368072242f,-0.999322355f,0.0337411724f,-0.999430597f,0.030674804f,-0.999529421f,0.027608145f,-0.999618828f,
0.024541229f,-0.999698818f,0.0214740802f,-0.99976939f,0.0184067301f,-0.999830604f,0.015339206f,-0.99988234f,0.0122715384f,-0.999924719f,0.00920375437f,-0.999957621f,
0.00613588467f,-0.999981165f,0.00306795677f,-0.999995291f,0.f,-1.0f,-0.00306795677f,-0.999995291f,-0.00613588467f,-0.999981165f,-0.00920375437f,-0.999957621f,
-0.0122715384f,-0.999924719f,-0.015339206f,-0.99988234f,-0.0184067301f,-0.999830604f,-0.0214740802f,-0.99976939f,-0.024541229f,-0.999698818f,-0.027608145f,-0.999618828f,
-0.030674804f,-0.999529421f,-0.0337411724f,-0.999430597f,-0.0368072242f,-0.999322355f,-0.0398729257f,-0.999204755f,-0.0429382585f,-0.999077737f,-0.0460031815f,-0.998941302f,
-0.0490676761f,-0.99879545f,-0.052131705f,-0.998640239f,-0.0551952459f,-0.998475552f,-0.0582582653f,-0.998301566f,-0.061320737f,-0.998118103f,-0.0643826276f,-0.997925282f,
-0.0674439222f,-0.997723043f,-0.070504576f,-0.997511446f,-0.0735645667f,-0.997290432f,-0.0766238645f,-0.997060061f,-0.0796824396f,-0.996820271f,-0.0827402622f,-0.996571124f,
-0.0857973099f,-0.996312618f,-0.0888535529f,-0.996044695f,-0.0919089541f,-0.995767415f,-0.0949634984f,-0.995480776f,-0.0980171412f,-0.99518472f,-0.10106986f,-0.994879305f,
-0.104121633f,-0.994564593f,-0.107172422f,-0.994240463f,-0.110222206f,-0.993906975f,-0.113270953f,-0.993564129f,-0.116318628f,-0.993211925f,-0.119365215f,-0.992850423f,
-0.122410677f,-0.992479563f,-0.125454977f,-0.992099285f,-0.128498107f,-0.991709769f,-0.13154003f,-0.991310835f,-0.134580702f,-0.990902662f,-0.137620121f,-0.990485072f,
-0.140658244f,-0.990058184f,-0.143695027f,-0.989621997f,-0.146730468f,-0.989176512f,-0.149764538f,-0.988721669f,-0.152797192f,-0.988257587f,-0.155828401f,-0.987784147f,
-0.15885815f,-0.987301409f,-0.161886394f,-0.986809373f,-0.164913118f,-0.986308098f,-0.167938292f,-0.985797524f,-0.170961887f,-0.985277653f,-0.173983872f,-0.984748483f,
-0.177004218f,-0.984210074f,-0.180022895f,-0.983662426f,-0.183039889f,-0.983105481f,-0.186055154f,-0.982539296f,-0.18906866f,-0.981963873f,-0.192080393f,-0.981379211f,
-0.195090324f,-0.980785251f,-0.198098406f,-0.980182111f,-0.201104641f,-0.979569793f,-0.204108968f,-0.978948176f,-0.207111374f,-0.97831738f,-0.210111842f,-0.977677345f,
-0.213110313f,-0.977028131f,-0.216106802f,-0.976369739f,-0.219101235f,-0.975702107f,-0.222093627f,-0.975025356f,-0.225083917f,-0.974339366f,-0.228072077f,-0.973644257f,
-0.231058106f,-0.972939968f,-0.234041959f,-0.972226501f,-0.237023607f,-0.971503913f,-0.24000302f,-0.970772147f,-0.242980182f,-0.970031261f,-0.24595505f,-0.969281256f,
-0.248927608f,-0.968522072f,-0.251897812f,-0.967753828f,-0.254865646f,-0.966976464f,-0.257831097f,-0.966189981f,-0.260794103f,-0.965394437f,-0.263754666f,-0.964589775f,
-0.266712755f,-0.963776052f,-0.269668311f,-0.962953269f,-0.272621363f,-0.962121427f,-0.275571823f,-0.961280465f,-0.27851969f,-0.960430503f,-0.281464934f,-0.95957154f,
-0.284407526f,-0.958703458f,-0.287347466f,-0.957826436f,-0.290284663f,-0.956940353f,-0.293219149f,-0.95604527f,-0.296150893f,-0.955141187f,-0.299079835f,-0.954228103f,
-0.302005947f,-0.953306019f,-0.304929227f,-0.952374995f,-0.307849646f,-0.95143503f,-0.310767144f,-0.950486064f,-0.313681751f,-0.949528158f,-0.316593379f,-0.94856137f,
-0.319502026f,-0.947585583f,-0.322407693f,-0.946600914f,-0.32531029f,-0.945607305f,-0.328209847f,-0.944604814f,-0.331106305f,-0.943593442f,-0.333999664f,-0.94257319f,
-0.336889863f,-0.941544056f,-0.339776874f,-0.940506041f,-0.342660725f,-0.939459205f,-0.345541328f,-0.938403547f,-0.348418683f,-0.937339008f,-0.351292759f,-0.936265647f,
-0.354163527f,-0.935183525f,-0.357030958f,-0.934092522f,-0.359895051f,-0.932992816f,-0.362755716f,-0.931884289f,-0.365612984f,-0.93076694f,-0.368466824f,-0.929640889f,
-0.371317208f,-0.928506076f,-0.374164075f,-0.927362502f,-0.377007425f,-0.926210225f,-0.379847199f,-0.925049245f,-0.382683426f,-0.923879504f,-0.385516047f,-0.92270112f,
-0.388345033f,-0.921514034f,-0.391170382f,-0.920318305f,-0.393992037f,-0.919113874f,-0.396809995f,-0.917900801f,-0.399624199f,-0.916679084f,-0.402434647f,-0.915448725f,
-0.405241311f,-0.914209783f,-0.408044159f,-0.912962198f,-0.410843164f,-0.91170603f,-0.413638324f,-0.910441279f,-0.416429549f,-0.909168005f,-0.419216901f,-0.907886088f,
-0.422000259f,-0.906595707f,-0.424779683f,-0.905296743f,-0.427555084f,-0.903989315f,-0.430326492f,-0.902673304f,-0.433093816f,-0.901348829f,-0.435857087f,-0.900015891f,
-0.438616246f,-0.898674488f,-0.441371262f,-0.897324562f,-0.444122136f,-0.895966232f,-0.446868837f,-0.894599497f,-0.449611336f,-0.893224299f,-0.452349573f,-0.891840696f,
-0.455083579f,-0.890448749f,-0.457813293f,-0.889048338f,-0.460538715f,-0.887639642f,-0.463259786f,-0.886222541f,-0.465976506f,-0.884797096f,-0.468688816f,-0.883363366f,
-0.471396744f,-0.881921291f,-0.474100202f,-0.880470872f,-0.47679922f,-0.879012227f,-0.479493767f,-0.877545297f,-0.482183784f,-0.876070082f,-0.484869242f,-0.874586642f,
-0.487550169f,-0.873094976f,-0.490226477f,-0.871595085f,-0.492898196f,-0.870086968f,-0.495565265f,-0.868570685f,-0.498227656f,-0.867046237f,-0.500885367f,-0.865513623f,
-0.50353837f,-0.863972843f,-0.506186664f,-0.862423956f,-0.50883013f,-0.860866964f,-0.511468828f,-0.859301805f,-0.514102757f,-0.857728601f,-0.516731799f,-0.856147349f,
-0.519356012f,-0.854557991f,-0.521975279f,-0.852960587f,-0.524589658f,-0.851355195f,-0.527199149f,-0.849741757f,-0.529803634f,-0.848120332f,-0.532403111f,-0.84649092f,
-0.534997642f,-0.84485358f,-0.537587047f,-0.843208253f,-0.540171444f,-0.841554999f,-0.542750776f,-0.839893818f,-0.545324981f,-0.838224709f,-0.547894061f,-0.836547732f,
-0.550457954f,-0.834862888f,-0.553016722f,-0.833170176f,-0.555570245f,-0.831469595f,-0.558118522f,-0.829761207f,-0.560661554f,-0.82804507f,-0.563199341f,-0.826321065f,
-0.565731823f,-0.824589312f,-0.568258941f,-0.82284981f,-0.570780754f,-0.8211025f,-0.573297143f,-0.819347501f,-0.575808167f,-0.817584813f,-0.578313768f,-0.815814435f,
-0.580813944f,-0.81403631f,-0.583308637f,-0.812250614f,-0.585797846f,-0.81045717f,-0.588281572f,-0.808656156f,-0.590759695f,-0.806847572f,-0.593232274f,-0.805031359f,
-0.59569931f,-0.803207517f,-0.598160684f,-0.801376164f,-0.600616455f,-0.799537241f,-0.603066623f,-0.797690868f,-0.605511069f,-0.795836926f,-0.607949793f,-0.793975472f,
-0.610382795f,-0.792106569f,-0.612810075f,-0.790230215f,-0.615231574f,-0.78834641f,-0.61764729f,-0.786455214f,-0.620057225f,-0.784556568f,-0.622461259f,-0.78265059f,
-0.624859512f,-0.780737221f,-0.627251804f,-0.778816521f,-0.629638255f,-0.77688849f,-0.632018745f,-0.774953127f,-0.634393275f,-0.773010433f,-0.636761844f,-0.771060526f,
-0.639124453f,-0.769103348f,-0.641481042f,-0.767138898f,-0.643831551f,-0.765167236f,-0.64617604f,-0.763188422f,-0.64851439f,-0.761202395f,-0.65084666f,-0.759209216f,
-0.653172851f,-0.757208824f,-0.655492842f,-0.755201399f,-0.657806695f,-0.753186822f,-0.660114348f,-0.751165152f,-0.662415802f,-0.749136388f,-0.664710999f,-0.747100592f,
-0.666999936f,-0.745057762f,-0.669282615f,-0.743007958f,-0.671558976f,-0.740951121f,-0.673829019f,-0.73888731f,-0.676092684f,-0.736816585f,-0.678350031f,-0.734738886f,
-0.680601001f,-0.732654274f,-0.682845533f,-0.730562747f,-0.685083687f,-0.728464365f,-0.687315345f,-0.726359129f,-0.689540565f,-0.724247098f,-0.691759229f,-0.722128212f,
-0.693971455f,-0.720002532f,-0.696177125f,-0.717870057f,-0.698376238f,-0.715730846f,-0.700568795f,-0.71358484f,-0.702754736f,-0.711432219f,-0.704934061f,-0.709272802f,
-0.707106769f,-0.707106769f,-0.709272802f,-0.704934061f,-0.711432219f,-0.702754736f,-0.71358484f,-0.700568795f,-0.715730846f,-0.698376238f,-0.717870057f,-0.696177125f,
-0.720002532f,-0.693971455f,-0.722128212f,-0.691759229f,-0.724247098f,-0.689540565f,-0.726359129f,-0.687315345f,-0.728464365f,-0.685083687f,-0.730562747f,-0.682845533f,
-0.732654274f,-0.680601001f,-0.734738886f,-0.678350031f,-0.736816585f,-0.676092684f,-0.73888731f,-0.673829019f,-0.740951121f,-0.671558976f,-0.743007958f,-0.669282615f,
-0.745057762f,-0.666999936f,-0.747100592f,-0.664710999f,-0.749136388f,-0.662415802f,-0.751165152f,-0.660114348f,-0.753186822f,-0.657806695f,-0.755201399f,-0.655492842f,
-0.757208824f,-0.653172851f,-0.759209216f,-0.65084666f,-0.761202395f,-0.64851439f,-0.763188422f,-0.64617604f,-0.765167236f,-0.643831551f,-0.767138898f,-0.641481042f,
-0.769103348f,-0.639124453f,-0.771060526f,-0.636761844f,-0.773010433f,-0.634393275f,-0.774953127f,-0.632018745f,-0.77688849f,-0.629638255f,-0.778816521f,-0.627251804f,
-0.780737221f,-0.624859512f,-0.78265059f,-0.622461259f,-0.784556568f,-0.620057225f,-0.786455214f,-0.61764729f,-0.78834641f,-0.615231574f,-0.790230215f,-0.612810075f,
-0.792106569f,-0.610382795f,-0.793975472f,-0.607949793f,-0.795836926f,-0.605511069f,-0.797690868f,-0.603066623f,-0.799537241f,-0.600616455f,-0.801376164f,-0.598160684f,
-0.803207517f,-0.59569931f,-0.805031359f,-0.593232274f,-0.806847572f,-0.590759695f,-0.808656156f,-0.588281572f,-0.81045717f,-0.585797846f,-0.812250614f,-0.583308637f,
-0.81403631f,-0.580813944f,-0.815814435f,-0.578313768f,-0.817584813f,-0.575808167f,-0.819347501f,-0.573297143f,-0.8211025f,-0.570780754f,-0.82284981f,-0.568258941f,
-0.824589312f,-0.565731823f,-0.826321065f,-0.563199341f,-0.82804507f,-0.560661554f,-0.829761207f,-0.558118522f,-0.831469595f,-0.555570245f,-0.833170176f,-0.553016722f,
-0.834862888f,-0.550457954f,-0.836547732f,-0.547894061f,-0.838224709f,-0.545324981f,-0.839893818f,-0.542750776f,-0.841554999f,-0.540171444f,-0.843208253f,-0.537587047f,
-0.84485358f,-0.534997642f,-0.84649092f,-0.532403111f,-0.848120332f,-0.529803634f,-0.849741757f,-0.527199149f,-0.851355195f,-0.524589658f,-0.852960587f,-0.521975279f,
-0.854557991f,-0.519356012f,-0.856147349f,-0.516731799f,-0.857728601f,-0.514102757f,-0.859301805f,-0.511468828f,-0.860866964f,-0.50883013f,-0.862423956f,-0.506186664f,
-0.863972843f,-0.50353837f,-0.865513623f,-0.500885367f,-0.867046237f,-0.498227656f,-0.868570685f,-0.495565265f,-0.870086968f,-0.492898196f,-0.871595085f,-0.490226477f,
-0.873094976f,-0.487550169f,-0.874586642f,-0.484869242f,-0.876070082f,-0.482183784f,-0.877545297f,-0.479493767f,-0.879012227f,-0.47679922f,-0.880470872f,-0.474100202f,
-0.881921291f,-0.471396744f,-0.883363366f,-0.468688816f,-0.884797096f,-0.465976506f,-0.886222541f,-0.463259786f,-0.887639642f,-0.460538715f,-0.889048338f,-0.457813293f,
-0.890448749f,-0.455083579f,-0.891840696f,-0.452349573f,-0.893224299f,-0.449611336f,-0.894599497f,-0.446868837f,-0.895966232f,-0.444122136f,-0.897324562f,-0.441371262f,
-0.898674488f,-0.438616246f,-0.900015891f,-0.435857087f,-0.901348829f,-0.433093816f,-0.902673304f,-0.430326492f,-0.903989315f,-0.427555084f,-0.905296743f,-0.424779683f,
-0.906595707f,-0.422000259f,-0.907886088f,-0.419216901f,-0.909168005f,-0.416429549f,-0.910441279f,-0.413638324f,-0.91170603f,-0.410843164f,-0.912962198f,-0.408044159f,
-0.914209783f,-0.405241311f,-0.915448725f,-0.402434647f,-0.916679084f,-0.399624199f,-0.917900801f,-0.396809995f,-0.919113874f,-0.393992037f,-0.920318305f,-0.391170382f,
-0.921514034f,-0.388345033f,-0.92270112f,-0.385516047f,-0.923879504f,-0.382683426f,-0.925049245f,-0.379847199f,-0.926210225f,-0.377007425f,-0.927362502f,-0.374164075f,
-0.928506076f,-0.371317208f,-0.929640889f,-0.368466824f,-0.93076694f,-0.365612984f,-0.931884289f,-0.362755716f,-0.932992816f,-0.359895051f,-0.934092522f,-0.357030958f,
-0.935183525f,-0.354163527f,-0.936265647f,-0.351292759f,-0.937339008f,-0.348418683f,-0.938403547f,-0.345541328f,-0.939459205f,-0.342660725f,-0.940506041f,-0.339776874f,
-0.941544056f,-0.336889863f,-0.94257319f,-0.333999664f,-0.943593442f,-0.331106305f,-0.944604814f,-0.328209847f,-0.945607305f,-0.32531029f,-0.946600914f,-0.322407693f,
-0.947585583f,-0.319502026f,-0.94856137f,-0.316593379f,-0.949528158f,-0.313681751f,-0.950486064f,-0.310767144f,-0.95143503f,-0.307849646f,-0.952374995f,-0.304929227f,
-0.953306019f,-0.302005947f,-0.954228103f,-0.299079835f,-0.955141187f,-0.296150893f,-0.95604527f,-0.293219149f,-0.956940353f,-0.290284663f,-0.957826436f,-0.287347466f,
-0.958703458f,-0.284407526f,-0.95957154f,-0.281464934f,-0.960430503f,-0.27851969f,-0.961280465f,-0.275571823f,-0.962121427f,-0.272621363f,-0.962953269f,-0.269668311f,
-0.963776052f,-0.266712755f,-0.964589775f,-0.263754666f,-0.965394437f,-0.260794103f,-0.966189981f,-0.257831097f,-0.966976464f,-0.254865646f,-0.967753828f,-0.251897812f,
-0.968522072f,-0.248927608f,-0.969281256f,-0.24595505f,-0.970031261f,-0.242980182f,-0.970772147f,-0.24000302f,-0.971503913f,-0.237023607f,-0.972226501f,-0.234041959f,
-0.972939968f,-0.231058106f,-0.973644257f,-0.228072077f,-0.974339366f,-0.225083917f,-0.975025356f,-0.222093627f,-0.975702107f,-0.219101235f,-0.976369739f,-0.216106802f,
-0.977028131f,-0.213110313f,-0.977677345f,-0.210111842f,-0.97831738f,-0.207111374f,-0.978948176f,-0.204108968f,-0.979569793f,-0.201104641f,-0.980182111f,-0.198098406f,
-0.980785251f,-0.195090324f,-0.981379211f,-0.192080393f,-0.981963873f,-0.18906866f,-0.982539296f,-0.186055154f,-0.983105481f,-0.183039889f,-0.983662426f,-0.180022895f,
-0.984210074f,-0.177004218f,-0.984748483f,-0.173983872f,-0.985277653f,-0.170961887f,-0.985797524f,-0.167938292f,-0.986308098f,-0.164913118f,-0.986809373f,-0.161886394f,
-0.987301409f,-0.15885815f,-0.987784147f,-0.155828401f,-0.988257587f,-0.152797192f,-0.988721669f,-0.149764538f,-0.989176512f,-0.146730468f,-0.989621997f,-0.143695027f,
-0.990058184f,-0.140658244f,-0.990485072f,-0.137620121f,-0.990902662f,-0.134580702f,-0.991310835f,-0.13154003f,-0.991709769f,-0.128498107f,-0.992099285f,-0.125454977f,
-0.992479563f,-0.122410677f,-0.992850423f,-0.119365215f,-0.993211925f,-0.116318628f,-0.993564129f,-0.113270953f,-0.993906975f,-0.110222206f,-0.994240463f,-0.107172422f,
-0.994564593f,-0.104121633f,-0.994879305f,-0.10106986f,-0.99518472f,-0.0980171412f,-0.995480776f,-0.0949634984f,-0.995767415f,-0.0919089541f,-0.996044695f,-0.0888535529f,
-0.996312618f,-0.0857973099f,-0.996571124f,-0.0827402622f,-0.996820271f,-0.0796824396f,-0.997060061f,-0.0766238645f,-0.997290432f,-0.0735645667f,-0.997511446f,-0.070504576f,
-0.997723043f,-0.0674439222f,-0.997925282f,-0.0643826276f,-0.998118103f,-0.061320737f,-0.998301566f,-0.0582582653f,-0.998475552f,-0.0551952459f,-0.998640239f,-0.052131705f,
-0.99879545f,-0.0490676761f,-0.998941302f,-0.0460031815f,-0.999077737f,-0.0429382585f,-0.999204755f,-0.0398729257f,-0.999322355f,-0.0368072242f,-0.999430597f,-0.0337411724f,
-0.999529421f,-0.030674804f,-0.999618828f,-0.027608145f,-0.999698818f,-0.024541229f,-0.99976939f,-0.0214740802f,-0.999830604f,-0.0184067301f,-0.99988234f,-0.015339206f,
-0.999924719f,-0.0122715384f,-0.999957621f,-0.00920375437f,-0.999981165f,-0.00613588467f,-0.999995291f,-0.00306795677f,-1.0f,0.f,-0.999995291f,0.00306795677f,
-0.999981165f,0.00613588467f,-0.999957621f,0.00920375437f,-0.999924719f,0.0122715384f,-0.99988234f,0.015339206f,-0.999830604f,0.0184067301f,-0.99976939f,0.0214740802f,
-0.999698818f,0.024541229f,-0.999618828f,0.027608145f,-0.999529421f,0.030674804f,-0.999430597f,0.0337411724f,-0.999322355f,0.0368072242f,-0.999204755f,0.0398729257f,
-0.999077737f,0.0429382585f,-0.998941302f,0.0460031815f,-0.99879545f,0.0490676761f,-0.998640239f,0.052131705f,-0.998475552f,0.0551952459f,-0.998301566f,0.0582582653f,
-0.998118103f,0.061320737f,-0.997925282f,0.0643826276f,-0.997723043f,0.0674439222f,-0.997511446f,0.070504576f,-0.997290432f,0.0735645667f,-0.997060061f,0.0766238645f,
-0.996820271f,0.0796824396f,-0.996571124f,0.0827402622f,-0.996312618f,0.0857973099f,-0.996044695f,0.0888535529f,-0.995767415f,0.0919089541f,-0.995480776f,0.0949634984f,
-0.99518472f,0.0980171412f,-0.994879305f,0.10106986f,-0.994564593f,0.104121633f,-0.994240463f,0.107172422f,-0.993906975f,0.110222206f,-0.993564129f,0.113270953f,
-0.993211925f,0.116318628f,-0.992850423f,0.119365215f,-0.992479563f,0.122410677f,-0.992099285f,0.125454977f,-0.991709769f,0.128498107f,-0.991310835f,0.13154003f,
-0.990902662f,0.134580702f,-0.990485072f,0.137620121f,-0.990058184f,0.140658244f,-0.989621997f,0.143695027f,-0.989176512f,0.146730468f,-0.988721669f,0.149764538f,
-0.988257587f,0.152797192f,-0.987784147f,0.155828401f,-0.987301409f,0.15885815f,-0.986809373f,0.161886394f,-0.986308098f,0.164913118f,-0.985797524f,0.167938292f,
-0.985277653f,0.170961887f,-0.984748483f,0.173983872f,-0.984210074f,0.177004218f,-0.983662426f,0.180022895f,-0.983105481f,0.183039889f,-0.982539296f,0.186055154f,
-0.981963873f,0.18906866f,-0.981379211f,0.192080393f,-0.980785251f,0.195090324f,-0.980182111f,0.198098406f,-0.979569793f,0.201104641f,-0.978948176f,0.204108968f,
-0.97831738f,0.207111374f,-0.977677345f,0.210111842f,-0.977028131f,0.213110313f,-0.976369739f,0.216106802f,-0.975702107f,0.219101235f,-0.975025356f,0.222093627f,
-0.974339366f,0.225083917f,-0.973644257f,0.228072077f,-0.972939968f,0.231058106f,-0.972226501f,0.234041959f,-0.971503913f,0.237023607f,-0.970772147f,0.24000302f,
-0.970031261f,0.242980182f,-0.969281256f,0.24595505f,-0.968522072f,0.248927608f,-0.967753828f,0.251897812f,-0.966976464f,0.254865646f,-0.966189981f,0.257831097f,
-0.965394437f,0.260794103f,-0.964589775f,0.263754666f,-0.963776052f,0.266712755f,-0.962953269f,0.269668311f,-0.962121427f,0.272621363f,-0.961280465f,0.275571823f,
-0.960430503f,0.27851969f,-0.95957154f,0.281464934f,-0.958703458f,0.284407526f,-0.957826436f,0.287347466f,-0.956940353f,0.290284663f,-0.95604527f,0.293219149f,
-0.955141187f,0.296150893f,-0.954228103f,0.299079835f,-0.953306019f,0.302005947f,-0.952374995f,0.304929227f,-0.95143503f,0.307849646f,-0.950486064f,0.310767144f,
-0.949528158f,0.313681751f,-0.94856137f,0.316593379f,-0.947585583f,0.319502026f,-0.946600914f,0.322407693f,-0.945607305f,0.32531029f,-0.944604814f,0.328209847f,
-0.943593442f,0.331106305f,-0.94257319f,0.333999664f,-0.941544056f,0.336889863f,-0.940506041f,0.339776874f,-0.939459205f,0.342660725f,-0.938403547f,0.345541328f,
-0.937339008f,0.348418683f,-0.936265647f,0.351292759f,-0.935183525f,0.354163527f,-0.934092522f,0.357030958f,-0.932992816f,0.359895051f,-0.931884289f,0.362755716f,
-0.93076694f,0.365612984f,-0.929640889f,0.368466824f,-0.928506076f,0.371317208f,-0.927362502f,0.374164075f,-0.926210225f,0.377007425f,-0.925049245f,0.379847199f,
-0.923879504f,0.382683426f,-0.92270112f,0.385516047f,-0.921514034f,0.388345033f,-0.920318305f,0.391170382f,-0.919113874f,0.393992037f,-0.917900801f,0.396809995f,
-0.916679084f,0.399624199f,-0.915448725f,0.402434647f,-0.914209783f,0.405241311f,-0.912962198f,0.408044159f,-0.91170603f,0.410843164f,-0.910441279f,0.413638324f,
-0.909168005f,0.416429549f,-0.907886088f,0.419216901f,-0.906595707f,0.422000259f,-0.905296743f,0.424779683f,-0.903989315f,0.427555084f,-0.902673304f,0.430326492f,
-0.901348829f,0.433093816f,-0.900015891f,0.435857087f,-0.898674488f,0.438616246f,-0.897324562f,0.441371262f,-0.895966232f,0.444122136f,-0.894599497f,0.446868837f,
-0.893224299f,0.449611336f,-0.891840696f,0.452349573f,-0.890448749f,0.455083579f,-0.889048338f,0.457813293f,-0.887639642f,0.460538715f,-0.886222541f,0.463259786f,
-0.884797096f,0.465976506f,-0.883363366f,0.468688816f,-0.881921291f,0.471396744f,-0.880470872f,0.474100202f,-0.879012227f,0.47679922f,-0.877545297f,0.479493767f,
-0.876070082f,0.482183784f,-0.874586642f,0.484869242f,-0.873094976f,0.487550169f,-0.871595085f,0.490226477f,-0.870086968f,0.492898196f,-0.868570685f,0.495565265f,
-0.867046237f,0.498227656f,-0.865513623f,0.500885367f,-0.863972843f,0.50353837f,-0.862423956f,0.506186664f,-0.860866964f,0.50883013f,-0.859301805f,0.511468828f,
-0.857728601f,0.514102757f,-0.856147349f,0.516731799f,-0.854557991f,0.519356012f,-0.852960587f,0.521975279f,-0.851355195f,0.524589658f,-0.849741757f,0.527199149f,
-0.848120332f,0.529803634f,-0.84649092f,0.532403111f,-0.84485358f,0.534997642f,-0.843208253f,0.537587047f,-0.841554999f,0.540171444f,-0.839893818f,0.542750776f,
-0.838224709f,0.545324981f,-0.836547732f,0.547894061f,-0.834862888f,0.550457954f,-0.833170176f,0.553016722f,-0.831469595f,0.555570245f,-0.829761207f,0.558118522f,
-0.82804507f,0.560661554f,-0.826321065f,0.563199341f,-0.824589312f,0.565731823f,-0.82284981f,0.568258941f,-0.8211025f,0.570780754f,-0.819347501f,0.573297143f,
-0.817584813f,0.575808167f,-0.815814435f,0.578313768f,-0.81403631f,0.580813944f,-0.812250614f,0.583308637f,-0.81045717f,0.585797846f,-0.808656156f,0.588281572f,
-0.806847572f,0.590759695f,-0.805031359f,0.593232274f,-0.803207517f,0.59569931f,-0.801376164f,0.598160684f,-0.799537241f,0.600616455f,-0.797690868f,0.603066623f,
-0.795836926f,0.605511069f,-0.793975472f,0.607949793f,-0.792106569f,0.610382795f,-0.790230215f,0.612810075f,-0.78834641f,0.615231574f,-0.786455214f,0.61764729f,
-0.784556568f,0.620057225f,-0.78265059f,0.622461259f,-0.780737221f,0.624859512f,-0.778816521f,0.627251804f,-0.77688849f,0.629638255f,-0.774953127f,0.632018745f,
-0.773010433f,0.634393275f,-0.771060526f,0.636761844f,-0.769103348f,0.639124453f,-0.767138898f,0.641481042f,-0.765167236f,0.643831551f,-0.763188422f,0.64617604f,
-0.761202395f,0.64851439f,-0.759209216f,0.65084666f,-0.757208824f,0.653172851f,-0.755201399f,0.655492842f,-0.753186822f,0.657806695f,-0.751165152f,0.660114348f,
-0.749136388f,0.662415802f,-0.747100592f,0.664710999f,-0.745057762f,0.666999936f,-0.743007958f,0.669282615f,-0.740951121f,0.671558976f,-0.73888731f,0.673829019f,
-0.736816585f,0.676092684f,-0.734738886f,0.678350031f,-0.732654274f,0.680601001f,-0.730562747f,0.682845533f,-0.728464365f,0.685083687f,-0.726359129f,0.687315345f,
-0.724247098f,0.689540565f,-0.722128212f,0.691759229f,-0.720002532f,0.693971455f,-0.717870057f,0.696177125f,-0.715730846f,0.698376238f,-0.71358484f,0.700568795f,
-0.711432219f,0.702754736f,-0.709272802f,0.704934061f,-0.707106769f,0.707106769f,-0.704934061f,0.709272802f,-0.702754736f,0.711432219f,-0.700568795f,0.71358484f,
-0.698376238f,0.715730846f,-0.696177125f,0.717870057f,-0.693971455f,0.720002532f,-0.691759229f,0.722128212f,-0.689540565f,0.724247098f,-0.687315345f,0.726359129f,
-0.685083687f,0.728464365f,-0.682845533f,0.730562747f,-0.680601001f,0.732654274f,-0.678350031f,0.734738886f,-0.676092684f,0.736816585f,-0.673829019f,0.73888731f,
-0.671558976f,0.740951121f,-0.669282615f,0.743007958f,-0.666999936f,0.745057762f,-0.664710999f,0.747100592f,-0.662415802f,0.749136388f,-0.660114348f,0.751165152f,
-0.657806695f,0.753186822f,-0.655492842f,0.755201399f,-0.653172851f,0.757208824f,-0.65084666f,0.759209216f,-0.64851439f,0.761202395f,-0.64617604f,0.763188422f,
-0.643831551f,0.765167236f,-0.641481042f,0.767138898f,-0.639124453f,0.769103348f,-0.636761844f,0.771060526f,-0.634393275f,0.773010433f,-0.632018745f,0.774953127f,
-0.629638255f,0.77688849f,-0.627251804f,0.778816521f,-0.624859512f,0.780737221f,-0.622461259f,0.78265059f,-0.620057225f,0.784556568f,-0.61764729f,0.786455214f,
-0.615231574f,0.78834641f,-0.612810075f,0.790230215f,-0.610382795f,0.792106569f,-0.607949793f,0.793975472f,-0.605511069f,0.795836926f,-0.603066623f,0.797690868f,
-0.600616455f,0.799537241f,-0.598160684f,0.801376164f,-0.59569931f,0.803207517f,-0.593232274f,0.805031359f,-0.590759695f,0.806847572f,-0.588281572f,0.808656156f,
-0.585797846f,0.81045717f,-0.583308637f,0.812250614f,-0.580813944f,0.81403631f,-0.578313768f,0.815814435f,-0.575808167f,0.817584813f,-0.573297143f,0.819347501f,
-0.570780754f,0.8211025f,-0.568258941f,0.82284981f,-0.565731823f,0.824589312f,-0.563199341f,0.826321065f,-0.560661554f,0.82804507f,-0.558118522f,0.829761207f,
-0.555570245f,0.831469595f,-0.553016722f,0.833170176f,-0.550457954f,0.834862888f,-0.547894061f,0.836547732f,-0.545324981f,0.838224709f,-0.542750776f,0.839893818f,
-0.540171444f,0.841554999f,-0.537587047f,0.843208253f,-0.534997642f,0.84485358f,-0.532403111f,0.84649092f,-0.529803634f,0.848120332f,-0.527199149f,0.849741757f,
-0.524589658f,0.851355195f,-0.521975279f,0.852960587f,-0.519356012f,0.854557991f,-0.516731799f,0.856147349f,-0.514102757f,0.857728601f,-0.511468828f,0.859301805f,
-0.50883013f,0.860866964f,-0.506186664f,0.862423956f,-0.50353837f,0.863972843f,-0.500885367f,0.865513623f,-0.498227656f,0.867046237f,-0.495565265f,0.868570685f,
-0.492898196f,0.870086968f,-0.490226477f,0.871595085f,-0.487550169f,0.873094976f,-0.484869242f,0.874586642f,-0.482183784f,0.876070082f,-0.479493767f,0.877545297f,
-0.47679922f,0.879012227f,-0.474100202f,0.880470872f,-0.471396744f,0.881921291f,-0.468688816f,0.883363366f,-0.465976506f,0.884797096f,-0.463259786f,0.886222541f,
-0.460538715f,0.887639642f,-0.457813293f,0.889048338f,-0.455083579f,0.890448749f,-0.452349573f,0.891840696f,-0.449611336f,0.893224299f,-0.446868837f,0.894599497f,
-0.444122136f,0.895966232f,-0.441371262f,0.897324562f,-0.438616246f,0.898674488f,-0.435857087f,0.900015891f,-0.433093816f,0.901348829f,-0.430326492f,0.902673304f,
-0.427555084f,0.903989315f,-0.424779683f,0.905296743f,-0.422000259f,0.906595707f,-0.419216901f,0.907886088f,-0.416429549f,0.909168005f,-0.413638324f,0.910441279f,
-0.410843164f,0.91170603f,-0.408044159f,0.912962198f,-0.405241311f,0.914209783f,-0.402434647f,0.915448725f,-0.399624199f,0.916679084f,-0.396809995f,0.917900801f,
-0.393992037f,0.919113874f,-0.391170382f,0.920318305f,-0.388345033f,0.921514034f,-0.385516047f,0.92270112f,-0.382683426f,0.923879504f,-0.379847199f,0.925049245f,
-0.377007425f,0.926210225f,-0.374164075f,0.927362502f,-0.371317208f,0.928506076f,-0.368466824f,0.929640889f,-0.365612984f,0.93076694f,-0.362755716f,0.931884289f,
-0.359895051f,0.932992816f,-0.357030958f,0.934092522f,-0.354163527f,0.935183525f,-0.351292759f,0.936265647f,-0.348418683f,0.937339008f,-0.345541328f,0.938403547f,
-0.342660725f,0.939459205f,-0.339776874f,0.940506041f,-0.336889863f,0.941544056f,-0.333999664f,0.94257319f,-0.331106305f,0.943593442f,-0.328209847f,0.944604814f,
-0.32531029f,0.945607305f,-0.322407693f,0.946600914f,-0.319502026f,0.947585583f,-0.316593379f,0.94856137f,-0.313681751f,0.949528158f,-0.310767144f,0.950486064f,
-0.307849646f,0.95143503f,-0.304929227f,0.952374995f,-0.302005947f,0.953306019f,-0.299079835f,0.954228103f,-0.296150893f,0.955141187f,-0.293219149f,0.95604527f,
-0.290284663f,0.956940353f,-0.287347466f,0.957826436f,-0.284407526f,0.958703458f,-0.281464934f,0.95957154f,-0.27851969f,0.960430503f,-0.275571823f,0.961280465f,
-0.272621363f,0.962121427f,-0.269668311f,0.962953269f,-0.266712755f,0.963776052f,-0.263754666f,0.964589775f,-0.260794103f,0.965394437f,-0.257831097f,0.966189981f,
-0.254865646f,0.966976464f,-0.251897812f,0.967753828f,-0.248927608f,0.968522072f,-0.24595505f,0.969281256f,-0.242980182f,0.970031261f,-0.24000302f,0.970772147f,
-0.237023607f,0.971503913f,-0.234041959f,0.972226501f,-0.231058106f,0.972939968f,-0.228072077f,0.973644257f,-0.225083917f,0.974339366f,-0.222093627f,0.975025356f,
-0.219101235f,0.975702107f,-0.216106802f,0.976369739f,-0.213110313f,0.977028131f,-0.210111842f,0.977677345f,-0.207111374f,0.97831738f,-0.204108968f,0.978948176f,
-0.201104641f,0.979569793f,-0.198098406f,0.980182111f,-0.195090324f,0.980785251f,-0.192080393f,0.981379211f,-0.18906866f,0.981963873f,-0.186055154f,0.982539296f,
-0.183039889f,0.983105481f,-0.180022895f,0.983662426f,-0.177004218f,0.984210074f,-0.173983872f,0.984748483f,-0.170961887f,0.985277653f,-0.167938292f,0.985797524f,
-0.164913118f,0.986308098f,-0.161886394f,0.986809373f,-0.15885815f,0.987301409f,-0.155828401f,0.987784147f,-0.152797192f,0.988257587f,-0.149764538f,0.988721669f,
-0.146730468f,0.989176512f,-0.143695027f,0.989621997f,-0.140658244f,0.990058184f,-0.137620121f,0.990485072f,-0.134580702f,0.990902662f,-0.13154003f,0.991310835f,
-0.128498107f,0.991709769f,-0.125454977f,0.992099285f,-0.122410677f,0.992479563f,-0.119365215f,0.992850423f,-0.116318628f,0.993211925f,-0.113270953f,0.993564129f,
-0.110222206f,0.993906975f,-0.107172422f,0.994240463f,-0.104121633f,0.994564593f,-0.10106986f,0.994879305f,-0.0980171412f,0.99518472f,-0.0949634984f,0.995480776f,
-0.0919089541f,0.995767415f,-0.0888535529f,0.996044695f,-0.0857973099f,0.996312618f,-0.0827402622f,0.996571124f,-0.0796824396f,0.996820271f,-0.0766238645f,0.997060061f,
-0.0735645667f,0.997290432f,-0.070504576f,0.997511446f,-0.0674439222f,0.997723043f,-0.0643826276f,0.997925282f,-0.061320737f,0.998118103f,-0.0582582653f,0.998301566f,
-0.0551952459f,0.998475552f,-0.052131705f,0.998640239f,-0.0490676761f,0.99879545f,-0.0460031815f,0.998941302f,-0.0429382585f,0.999077737f,-0.0398729257f,0.999204755f,
-0.0368072242f,0.999322355f,-0.0337411724f,0.999430597f,-0.030674804f,0.999529421f,-0.027608145f,0.999618828f,-0.024541229f,0.999698818f,-0.0214740802f,0.99976939f,
-0.0184067301f,0.999830604f,-0.015339206f,0.99988234f,-0.0122715384f,0.999924719f,-0.00920375437f,0.999957621f,-0.00613588467f,0.999981165f,-0.00306795677f,0.999995291f,
0.f,1.0f,0.00306795677f,0.999995291f,0.00613588467f,0.999981165f,0.00920375437f,0.999957621f,0.0122715384f,0.999924719f,0.015339206f,0.99988234f,
0.0184067301f,0.999830604f,0.0214740802f,0.99976939f,0.024541229f,0.999698818f,0.027608145f,0.999618828f,0.030674804f,0.999529421f,0.0337411724f,0.999430597f,
0.0368072242f,0.999322355f,0.0398729257f,0.999204755f,0.0429382585f,0.999077737f,0.0460031815f,0.998941302f,0.0490676761f,0.99879545f,0.052131705f,0.998640239f,
0.0551952459f,0.998475552f,0.0582582653f,0.998301566f,0.061320737f,0.998118103f,0.0643826276f,0.997925282f,0.0674439222f,0.997723043f,0.070504576f,0.997511446f,
0.0735645667f,0.997290432f,0.0766238645f,0.997060061f,0.0796824396f,0.996820271f,0.0827402622f,0.996571124f,0.0857973099f,0.996312618f,0.0888535529f,0.996044695f,
0.0919089541f,0.995767415f,0.0949634984f,0.995480776f,0.0980171412f,0.99518472f,0.10106986f,0.994879305f,0.104121633f,0.994564593f,0.107172422f,0.994240463f,
0.110222206f,0.993906975f,0.113270953f,0.993564129f,0.116318628f,0.993211925f,0.119365215f,0.992850423f,0.122410677f,0.992479563f,0.125454977f,0.992099285f,
0.128498107f,0.991709769f,0.13154003f,0.991310835f,0.134580702f,0.990902662f,0.137620121f,0.990485072f,0.140658244f,0.990058184f,0.143695027f,0.989621997f,
0.146730468f,0.989176512f,0.149764538f,0.988721669f,0.152797192f,0.988257587f,0.155828401f,0.987784147f,0.15885815f,0.987301409f,0.161886394f,0.986809373f,
0.164913118f,0.986308098f,0.167938292f,0.985797524f,0.170961887f,0.985277653f,0.173983872f,0.984748483f,0.177004218f,0.984210074f,0.180022895f,0.983662426f,
0.183039889f,0.983105481f,0.186055154f,0.982539296f,0.18906866f,0.981963873f,0.192080393f,0.981379211f,0.195090324f,0.980785251f,0.198098406f,0.980182111f,
0.201104641f,0.979569793f,0.204108968f,0.978948176f,0.207111374f,0.97831738f,0.210111842f,0.977677345f,0.213110313f,0.977028131f,0.216106802f,0.976369739f,
0.219101235f,0.975702107f,0.222093627f,0.975025356f,0.225083917f,0.974339366f,0.228072077f,0.973644257f,0.231058106f,0.972939968f,0.234041959f,0.972226501f,
0.237023607f,0.971503913f,0.24000302f,0.970772147f,0.242980182f,0.970031261f,0.24595505f,0.969281256f,0.248927608f,0.968522072f,0.251897812f,0.967753828f,
0.254865646f,0.966976464f,0.257831097f,0.966189981f,0.260794103f,0.965394437f,0.263754666f,0.964589775f,0.266712755f,0.963776052f,0.269668311f,0.962953269f,
0.272621363f,0.962121427f,0.275571823f,0.961280465f,0.27851969f,0.960430503f,0.281464934f,0.95957154f,0.284407526f,0.958703458f,0.287347466f,0.957826436f,
0.290284663f,0.956940353f,0.293219149f,0.95604527f,0.296150893f,0.955141187f,0.299079835f,0.954228103f,0.302005947f,0.953306019f,0.304929227f,0.952374995f,
0.307849646f,0.95143503f,0.310767144f,0.950486064f,0.313681751f,0.949528158f,0.316593379f,0.94856137f,0.319502026f,0.947585583f,0.322407693f,0.946600914f,
0.32531029f,0.945607305f,0.328209847f,0.944604814f,0.331106305f,0.943593442f,0.333999664f,0.94257319f,0.336889863f,0.941544056f,0.339776874f,0.940506041f,
0.342660725f,0.939459205f,0.345541328f,0.938403547f,0.348418683f,0.937339008f,0.351292759f,0.936265647f,0.354163527f,0.935183525f,0.357030958f,0.934092522f,
0.359895051f,0.932992816f,0.362755716f,0.931884289f,0.365612984f,0.93076694f,0.368466824f,0.929640889f,0.371317208f,0.928506076f,0.374164075f,0.927362502f,
0.377007425f,0.926210225f,0.379847199f,0.925049245f,0.382683426f,0.923879504f,0.385516047f,0.92270112f,0.388345033f,0.921514034f,0.391170382f,0.920318305f,
0.393992037f,0.919113874f,0.396809995f,0.917900801f,0.399624199f,0.916679084f,0.402434647f,0.915448725f,0.405241311f,0.914209783f,0.408044159f,0.912962198f,
0.410843164f,0.91170603f,0.413638324f,0.910441279f,0.416429549f,0.909168005f,0.419216901f,0.907886088f,0.422000259f,0.906595707f,0.424779683f,0.905296743f,
0.427555084f,0.903989315f,0.430326492f,0.902673304f,0.433093816f,0.901348829f,0.435857087f,0.900015891f,0.438616246f,0.898674488f,0.441371262f,0.897324562f,
0.444122136f,0.895966232f,0.446868837f,0.894599497f,0.449611336f,0.893224299f,0.452349573f,0.891840696f,0.455083579f,0.890448749f,0.457813293f,0.889048338f,
0.460538715f,0.887639642f,0.463259786f,0.886222541f,0.465976506f,0.884797096f,0.468688816f,0.883363366f,0.471396744f,0.881921291f,0.474100202f,0.880470872f,
0.47679922f,0.879012227f,0.479493767f,0.877545297f,0.482183784f,0.876070082f,0.484869242f,0.874586642f,0.487550169f,0.873094976f,0.490226477f,0.871595085f,
0.492898196f,0.870086968f,0.495565265f,0.868570685f,0.498227656f,0.867046237f,0.500885367f,0.865513623f,0.50353837f,0.863972843f,0.506186664f,0.862423956f,
0.50883013f,0.860866964f,0.511468828f,0.859301805f,0.514102757f,0.857728601f,0.516731799f,0.856147349f,0.519356012f,0.854557991f,0.521975279f,0.852960587f,
0.524589658f,0.851355195f,0.527199149f,0.849741757f,0.529803634f,0.848120332f,0.532403111f,0.84649092f,0.534997642f,0.84485358f,0.537587047f,0.843208253f,
0.540171444f,0.841554999f,0.542750776f,0.839893818f,0.545324981f,0.838224709f,0.547894061f,0.836547732f,0.550457954f,0.834862888f,0.553016722f,0.833170176f,
0.555570245f,0.831469595f,0.558118522f,0.829761207f,0.560661554f,0.82804507f,0.563199341f,0.826321065f,0.565731823f,0.824589312f,0.568258941f,0.82284981f,
0.570780754f,0.8211025f,0.573297143f,0.819347501f,0.575808167f,0.817584813f,0.578313768f,0.815814435f,0.580813944f,0.81403631f,0.583308637f,0.812250614f,
0.585797846f,0.81045717f,0.588281572f,0.808656156f,0.590759695f,0.806847572f,0.593232274f,0.805031359f,0.59569931f,0.803207517f,0.598160684f,0.801376164f,
0.600616455f,0.799537241f,0.603066623f,0.797690868f,0.605511069f,0.795836926f,0.607949793f,0.793975472f,0.610382795f,0.792106569f,0.612810075f,0.790230215f,
0.615231574f,0.78834641f,0.61764729f,0.786455214f,0.620057225f,0.784556568f,0.622461259f,0.78265059f,0.624859512f,0.780737221f,0.627251804f,0.778816521f,
0.629638255f,0.77688849f,0.632018745f,0.774953127f,0.634393275f,0.773010433f,0.636761844f,0.771060526f,0.639124453f,0.769103348f,0.641481042f,0.767138898f,
0.643831551f,0.765167236f,0.64617604f,0.763188422f,0.64851439f,0.761202395f,0.65084666f,0.759209216f,0.653172851f,0.757208824f,0.655492842f,0.755201399f,
0.657806695f,0.753186822f,0.660114348f,0.751165152f,0.662415802f,0.749136388f,0.664710999f,0.747100592f,0.666999936f,0.745057762f,0.669282615f,0.743007958f,
0.671558976f,0.740951121f,0.673829019f,0.73888731f,0.676092684f,0.736816585f,0.678350031f,0.734738886f,0.680601001f,0.732654274f,0.682845533f,0.730562747f,
0.685083687f,0.728464365f,0.687315345f,0.726359129f,0.689540565f,0.724247098f,0.691759229f,0.722128212f,0.693971455f,0.720002532f,0.696177125f,0.717870057f,
0.698376238f,0.715730846f,0.700568795f,0.71358484f,0.702754736f,0.711432219f,0.704934061f,0.709272802f,0.707106769f,0.707106769f,0.709272802f,0.704934061f,
0.711432219f,0.702754736f,0.71358484f,0.700568795f,0.715730846f,0.698376238f,0.717870057f,0.696177125f,0.720002532f,0.693971455f,0.722128212f,0.691759229f,
0.724247098f,0.689540565f,0.726359129f,0.687315345f,0.728464365f,0.685083687f,0.730562747f,0.682845533f,0.732654274f,0.680601001f,0.734738886f,0.678350031f,
0.736816585f,0.676092684f,0.73888731f,0.673829019f,0.740951121f,0.671558976f,0.743007958f,0.669282615f,0.745057762f,0.666999936f,0.747100592f,0.664710999f,
0.749136388f,0.662415802f,0.751165152f,0.660114348f,0.753186822f,0.657806695f,0.755201399f,0.655492842f,0.757208824f,0.653172851f,0.759209216f,0.65084666f,
0.761202395f,0.64851439f,0.763188422f,0.64617604f,0.765167236f,0.643831551f,0.767138898f,0.641481042f,0.769103348f,0.639124453f,0.771060526f,0.636761844f,
0.773010433f,0.634393275f,0.774953127f,0.632018745f,0.77688849f,0.629638255f,0.778816521f,0.627251804f,0.780737221f,0.624859512f,0.78265059f,0.622461259f,
0.784556568f,0.620057225f,0.786455214f,0.61764729f,0.78834641f,0.615231574f,0.790230215f,0.612810075f,0.792106569f,0.610382795f,0.793975472f,0.607949793f,
0.795836926f,0.605511069f,0.797690868f,0.603066623f,0.799537241f,0.600616455f,0.801376164f,0.598160684f,0.803207517f,0.59569931f,0.805031359f,0.593232274f,
0.806847572f,0.590759695f,0.808656156f,0.588281572f,0.81045717f,0.585797846f,0.812250614f,0.583308637f,0.81403631f,0.580813944f,0.815814435f,0.578313768f,
0.817584813f,0.575808167f,0.819347501f,0.573297143f,0.8211025f,0.570780754f,0.82284981f,0.568258941f,0.824589312f,0.565731823f,0.826321065f,0.563199341f,
0.82804507f,0.560661554f,0.829761207f,0.558118522f,0.831469595f,0.555570245f,0.833170176f,0.553016722f,0.834862888f,0.550457954f,0.836547732f,0.547894061f,
0.838224709f,0.545324981f,0.839893818f,0.542750776f,0.841554999f,0.540171444f,0.843208253f,0.537587047f,0.84485358f,0.534997642f,0.84649092f,0.532403111f,
0.848120332f,0.529803634f,0.849741757f,0.527199149f,0.851355195f,0.524589658f,0.852960587f,0.521975279f,0.854557991f,0.519356012f,0.856147349f,0.516731799f,
0.857728601f,0.514102757f,0.859301805f,0.511468828f,0.860866964f,0.50883013f,0.862423956f,0.506186664f,0.863972843f,0.50353837f,0.865513623f,0.500885367f,
0.867046237f,0.498227656f,0.868570685f,0.495565265f,0.870086968f,0.492898196f,0.871595085f,0.490226477f,0.873094976f,0.487550169f,0.874586642f,0.484869242f,
0.876070082f,0.482183784f,0.877545297f,0.479493767f,0.879012227f,0.47679922f,0.880470872f,0.474100202f,0.881921291f,0.471396744f,0.883363366f,0.468688816f,
0.884797096f,0.465976506f,0.886222541f,0.463259786f,0.887639642f,0.460538715f,0.889048338f,0.457813293f,0.890448749f,0.455083579f,0.891840696f,0.452349573f,
0.893224299f,0.449611336f,0.894599497f,0.446868837f,0.895966232f,0.444122136f,0.897324562f,0.441371262f,0.898674488f,0.438616246f,0.900015891f,0.435857087f,
0.901348829f,0.433093816f,0.902673304f,0.430326492f,0.903989315f,0.427555084f,0.905296743f,0.424779683f,0.906595707f,0.422000259f,0.907886088f,0.419216901f,
0.909168005f,0.416429549f,0.910441279f,0.413638324f,0.91170603f,0.410843164f,0.912962198f,0.408044159f,0.914209783f,0.405241311f,0.915448725f,0.402434647f,
0.916679084f,0.399624199f,0.917900801f,0.396809995f,0.919113874f,0.393992037f,0.920318305f,0.391170382f,0.921514034f,0.388345033f,0.92270112f,0.385516047f,
0.923879504f,0.382683426f,0.925049245f,0.379847199f,0.926210225f,0.377007425f,0.927362502f,0.374164075f,0.928506076f,0.371317208f,0.929640889f,0.368466824f,
0.93076694f,0.365612984f,0.931884289f,0.362755716f,0.932992816f,0.359895051f,0.934092522f,0.357030958f,0.935183525f,0.354163527f,0.936265647f,0.351292759f,
0.937339008f,0.348418683f,0.938403547f,0.345541328f,0.939459205f,0.342660725f,0.940506041f,0.339776874f,0.941544056f,0.336889863f,0.94257319f,0.333999664f,
0.943593442f,0.331106305f,0.944604814f,0.328209847f,0.945607305f,0.32531029f,0.946600914f,0.322407693f,0.947585583f,0.319502026f,0.94856137f,0.316593379f,
0.949528158f,0.313681751f,0.950486064f,0.310767144f,0.95143503f,0.307849646f,0.952374995f,0.304929227f,0.953306019f,0.302005947f,0.954228103f,0.299079835f,
0.955141187f,0.296150893f,0.95604527f,0.293219149f,0.956940353f,0.290284663f,0.957826436f,0.287347466f,0.958703458f,0.284407526f,0.95957154f,0.281464934f,
0.960430503f,0.27851969f,0.961280465f,0.275571823f,0.962121427f,0.272621363f,0.962953269f,0.269668311f,0.963776052f,0.266712755f,0.964589775f,0.263754666f,
0.965394437f,0.260794103f,0.966189981f,0.257831097f,0.966976464f,0.254865646f,0.967753828f,0.251897812f,0.968522072f,0.248927608f,0.969281256f,0.24595505f,
0.970031261f,0.242980182f,0.970772147f,0.24000302f,0.971503913f,0.237023607f,0.972226501f,0.234041959f,0.972939968f,0.231058106f,0.973644257f,0.228072077f,
0.974339366f,0.225083917f,0.975025356f,0.222093627f,0.975702107f,0.219101235f,0.976369739f,0.216106802f,0.977028131f,0.213110313f,0.977677345f,0.210111842f,
0.97831738f,0.207111374f,0.978948176f,0.204108968f,0.979569793f,0.201104641f,0.980182111f,0.198098406f,0.980785251f,0.195090324f,0.981379211f,0.192080393f,
0.981963873f,0.18906866f,0.982539296f,0.186055154f,0.983105481f,0.183039889f,0.983662426f,0.180022895f,0.984210074f,0.177004218f,0.984748483f,0.173983872f,
0.985277653f,0.170961887f,0.985797524f,0.167938292f,0.986308098f,0.164913118f,0.986809373f,0.161886394f,0.987301409f,0.15885815f,0.987784147f,0.155828401f,
0.988257587f,0.152797192f,0.988721669f,0.149764538f,0.989176512f,0.146730468f,0.989621997f,0.143695027f,0.990058184f,0.140658244f,0.990485072f,0.137620121f,
0.990902662f,0.134580702f,0.991310835f,0.13154003f,0.991709769f,0.128498107f,0.992099285f,0.125454977f,0.992479563f,0.122410677f,0.992850423f,0.119365215f,
0.993211925f,0.116318628f,0.993564129f,0.113270953f,0.993906975f,0.110222206f,0.994240463f,0.107172422f,0.994564593f,0.104121633f,0.994879305f,0.10106986f,
0.99518472f,0.0980171412f,0.995480776f,0.0949634984f,0.995767415f,0.0919089541f,0.996044695f,0.0888535529f,0.996312618f,0.0857973099f,0.996571124f,0.0827402622f,
0.996820271f,0.0796824396f,0.997060061f,0.0766238645f,0.997290432f,0.0735645667f,0.997511446f,0.070504576f,0.997723043f,0.0674439222f,0.997925282f,0.0643826276f,
0.998118103f,0.061320737f,0.998301566f,0.0582582653f,0.998475552f,0.0551952459f,0.998640239f,0.052131705f,0.99879545f,0.0490676761f,0.998941302f,0.0460031815f,
0.999077737f,0.0429382585f,0.999204755f,0.0398729257f,0.999322355f,0.0368072242f,0.999430597f,0.0337411724f,0.999529421f,0.030674804f,0.999618828f,0.027608145f,
0.999698818f,0.024541229f,0.99976939f,0.0214740802f,0.999830604f,0.0184067301f,0.99988234f,0.015339206f,0.999924719f,0.0122715384f,0.999957621f,0.00920375437f,
0.999981165f,0.00613588467f,0.999995291f,0.00306795677f,
};

typedef short bf16x8 __attribute__((ext_vector_type(8)));
typedef short s16x4 __attribute__((ext_vector_type(4)));
typedef float f32x16 __attribute__((ext_vector_type(16)));
typedef float f32x4 __attribute__((ext_vector_type(4)));
typedef float f32x2 __attribute__((ext_vector_type(2)));
typedef unsigned u32x4 __attribute__((ext_vector_type(4)));
typedef unsigned u32x2 __attribute__((ext_vector_type(2)));
typedef __bf16 bf16x2_t __attribute__((ext_vector_type(2)));
#define SB() __builtin_amdgcn_sched_barrier(0)
#define LAS __attribute__((address_space(3)))
#define DI __device__ __forceinline__
#define MFMA32(a, b, c) __builtin_amdgcn_mfma_f32_32x32x16_bf16(a, b, c, 0, 0, 0)
#define MFMA16(a, b, c) __builtin_amdgcn_mfma_f32_16x16x32_bf16(a, b, c, 0, 0, 0)
#define WAIT_BAR0() asm volatile("s_waitcnt vmcnt(0) lgkmcnt(0)\n\ts_barrier" ::: "memory")
#define LBAR() asm volatile("s_waitcnt lgkmcnt(0)\n\ts_barrier" ::: "memory")

constexpr int SEQ = 2048, MD = 576, DC = 512, NH = 32, HD = 128;
constexpr int OUTW = 1152;
constexpr int TP = 296;
constexpr size_t WS_KV = 0;
constexpr size_t WS_CTX = WS_KV + (size_t)32 * 1024 * 1024;
constexpr size_t WS_WOF = WS_CTX + (size_t)16 * 1024 * 1024;
constexpr size_t WS_T = WS_WOF + (size_t)36 * 128 * 1024;
constexpr size_t WS_XCH = WS_T + (size_t)8 * 256 * TP * 8;
constexpr size_t WS_FLAG = WS_XCH + (size_t)64 * 16 * 18432;
constexpr size_t FL_E = 0, FL_KV = 128, FL_CTX = FL_KV + 256 * 128, FL_X = FL_CTX + 256 * 128, FL_S = FL_X + 256 * 128, FL_U = FL_S + 2048;
constexpr size_t WS_END = WS_FLAG + FL_U + 2048;
typedef unsigned long long u64;

DI unsigned cvtpk(float lo, float hi) {
    f32x2 v = {lo, hi}; bf16x2_t b = __builtin_convertvector(v, bf16x2_t); return __builtin_bit_cast(unsigned, b);
}
template <int S2> DI bf16x8 pack8(const f32x16& a) {
    u32x4 w = {cvtpk(a[8 * S2 + 0], a[8 * S2 + 1]), cvtpk(a[8 * S2 + 2], a[8 * S2 + 3]),
               cvtpk(a[8 * S2 + 4], a[8 * S2 + 5]), cvtpk(a[8 * S2 + 6], a[8 * S2 + 7])};
    return __builtin_bit_cast(bf16x8, w);
}
DI bf16x8 lds_rd128(unsigned a) { return *(const LAS bf16x8*)a; }
DI s16x4 lds_tr(unsigned a) { return __builtin_amdgcn_ds_read_tr16_b64_v4i16((LAS s16x4*)a); }
DI bf16x8 lds_tr2(unsigned a0, unsigned a1) {
    s16x4 lo = lds_tr(a0), hi = lds_tr(a1);
    return __builtin_shufflevector(lo, hi, 0, 1, 2, 3, 4, 5, 6, 7);
}
DI unsigned rfl(unsigned x) { return __builtin_amdgcn_readfirstlane(x); }
DI float swap32f(float x) {
    auto r = __builtin_amdgcn_permlane32_swap(__float_as_uint(x), __float_as_uint(x), false, false);
    const unsigned lane = threadIdx.x & 63;
    return __uint_as_float(lane < 32 ? r[1] : r[0]);
}

DI void glds(const void* sbase, unsigned voff, unsigned lds_addr) {
    asm volatile("s_mov_b32 m0, %2\n\ts_nop 0\n\tglobal_load_lds_dwordx4 %0, %1" :: "v"(voff), "s"(sbase), "s"(__builtin_amdgcn_readfirstlane(lds_addr)) : "memory");
}
DI void glds_sc1(const void* sbase, unsigned voff, unsigned lds_addr) {
    asm volatile("s_mov_b32 m0, %2\n\ts_nop 0\n\tglobal_load_lds_dwordx4 %0, %1 sc1" :: "v"(voff), "s"(sbase), "s"(__builtin_amdgcn_readfirstlane(lds_addr)) : "memory");
}
constexpr unsigned WRING = 98304;
struct KvFr { bf16x8 c[4]; s16x4 w[4]; };
template <int I> DI void kv_read(KvFr& f, unsigned ca, unsigned wb0, unsigned wb1) {
    if (I < 4) f.c[I] = lds_rd128(ca + I * 4096);
    else f.w[I - 4] = lds_tr(((I & 2) ? wb1 : wb0) + (I & 1) * 1024);
}
#define KVAUX 16
#define VM_BAR(n) asm volatile("s_waitcnt vmcnt(" #n ")\n\ts_barrier" ::: "memory")
template <int I> DI void kv_store1(const u32x4 (&pk)[16], __amdgpu_buffer_rsrc_t kvr, unsigned ovoff, int isv) {
    constexpr int kb = I >> 2, db = (I >> 1) & 1, s2 = I & 1;
    constexpr int fk = (kb & 1) * 8 + 2 * db + s2, fv = 16 + db * 4 + 2 * (kb & 1) + s2;
    __builtin_amdgcn_raw_buffer_store_b128(pk[I], kvr, ovoff + (kb >> 1) * 32768, (fk + isv * (fv - fk)) * 1024, KVAUX);
}
template <bool ISV, class ISS, class PF>
DI void kv_run(unsigned lds0, unsigned wx0, unsigned wx1, const unsigned (&cx)[4], ISS& iss, u32x4 (&pk)[16], PF pf) {
    f32x16 acc[4][2];
#pragma unroll
    for (int i = 0; i < 4; ++i) { acc[i][0] = f32x16{}; acc[i][1] = f32x16{}; }
    KvFr fa, fb;
    VM_BAR(8);
    { const unsigned w0 = lds0 + WRING + wx0, w1 = lds0 + WRING + wx1, ca = lds0 + cx[0];
      kv_read<4>(fa, 0, w0, w1); kv_read<5>(fa, 0, w0, w1); kv_read<0>(fa, ca, 0, 0); kv_read<6>(fa, 0, w0, w1); kv_read<7>(fa, 0, w0, w1);
      kv_read<1>(fa, ca, 0, 0); kv_read<2>(fa, ca, 0, 0); kv_read<3>(fa, ca, 0, 0); }
    int as = 0;
#define KVG(F, G, CA, W0, W1, I, RD, DM, GG) do { kv_read<RD>(G, CA, W0, W1); \
            acc[(I) >> 1][(I) & 1] = ISV ? MFMA32(F.c[(I) >> 1], ((I) & 1) ? w1 : w0, acc[(I) >> 1][(I) & 1]) : MFMA32(((I) & 1) ? w1 : w0, F.c[(I) >> 1], acc[(I) >> 1][(I) & 1]); \
            if ((DM) != 0 && ((I) & 1)) iss(GG, fa_, (I) >> 1, (DM)); \
            if ((DM) == 1 && (I) == 7) pf(GG, pd0); \
            SB(); } while (0)
#define KVS(F, G, CA, W0, W1, DM, GG) do { \
            const bf16x8 w0 = __builtin_shufflevector(F.w[0], F.w[1], 0, 1, 2, 3, 4, 5, 6, 7), w1 = __builtin_shufflevector(F.w[2], F.w[3], 0, 1, 2, 3, 4, 5, 6, 7); \
            KVG(F, G, CA, W0, W1, 0, 4, DM, GG); KVG(F, G, CA, W0, W1, 1, 5, DM, GG); KVG(F, G, CA, W0, W1, 2, 0, DM, GG); KVG(F, G, CA, W0, W1, 3, 6, DM, GG); \
            KVG(F, G, CA, W0, W1, 4, 7, DM, GG); KVG(F, G, CA, W0, W1, 5, 1, DM, GG); KVG(F, G, CA, W0, W1, 6, 2, DM, GG); KVG(F, G, CA, W0, W1, 7, 3, DM, GG); } while (0)
#define KVTILE(N0, N1, DMA, DMB) do { \
        const unsigned ab = lds0 + as * 32768, an = lds0 + (as == 2 ? 0 : as + 1) * 32768, fa_ = lds0 + (as == 0 ? 2 : as - 1) * 32768; \
        const unsigned wA = lds0 + WRING + ((2 * m) & 3) * 16384, wB = wA + 16384, wC = lds0 + WRING + ((2 * m + 2) & 3) * 16384; \
        VM_BAR(N0); SB(); \
        KVS(fa, fb, ab + cx[1], wA + wx0 + 4096, wA + wx1 + 4096, DMA, 2 * m); \
        KVS(fb, fa, ab + cx[2], wB + wx0, wB + wx1, 0, 0); \
        VM_BAR(N1); SB(); \
        KVS(fa, fb, ab + cx[3], wB + wx0 + 4096, wB + wx1 + 4096, DMB, 2 * m + 1); \
        KVS(fb, fa, an + cx[0], wC + wx0, wC + wx1, 0, 0);         \
        as = as == 2 ? 0 : as + 1; } while (0)
    unsigned pd0 = 0;
    { const int m = 0; KVTILE(4, 5, 1, 1); }
    for (int m = 1; m < 6; ++m) KVTILE(5, 5, 1, 1);
    { const int m = 6; KVTILE(5, 4, 2, 3); }
    { const int m = 7; KVTILE(3, 3, 3, 3); }
#undef KVTILE
#undef KVS
#undef KVG
#pragma unroll
    for (int kb = 0; kb < 4; ++kb)
#pragma unroll
        for (int db = 0; db < 2; ++db) {
            pk[(kb * 2 + db) * 2] = __builtin_bit_cast(u32x4, pack8<0>(acc[kb][db]));
            pk[(kb * 2 + db) * 2 + 1] = __builtin_bit_cast(u32x4, pack8<1>(acc[kb][db]));
        }
}

typedef f32x2 cf;
DI cf cmul(cf a, cf b) { return cf{a[0] * b[0] - a[1] * b[1], a[0] * b[1] + a[1] * b[0]}; }
DI cf cmulc(cf a, cf b) { return cf{a[0] * b[0] + a[1] * b[1], a[1] * b[0] - a[0] * b[1]}; }
template <bool INV> DI cf twm(cf a, cf t) { return INV ? cmulc(a, t) : cmul(a, t); }
template <bool INV> DI cf muli(cf a) { return INV ? cf{-a[1], a[0]} : cf{a[1], -a[0]}; }
DI cf tw576(int j) { return *(const cf*)(g_tw576 + 2 * j); }
DI cf tw2048(int j) { return *(const cf*)(g_tw2048 + 2 * j); }
template <bool INV> DI void dft3(cf& a0, cf& a1, cf& a2) {
    const cf t = a1 + a2, u = a1 - a2;
    const cf m = a0 - 0.5f * t, v = muli<INV>(u) * 0.8660254037844386f;
    a0 = a0 + t; a1 = m + v; a2 = m - v;
}
template <bool INV> DI void dft4(cf& a0, cf& a1, cf& a2, cf& a3) {
    const cf s0 = a0 + a2, d0 = a0 - a2, s1 = a1 + a3, d1 = muli<INV>(a1 - a3);
    a0 = s0 + s1; a1 = d0 + d1; a2 = s0 - s1; a3 = d0 - d1;
}
template <bool INV> DI void dft6(cf (&x)[6]) {
    cf a0 = x[0] + x[3], a1 = x[2] + x[5], a2 = x[4] + x[1];
    cf b0 = x[0] - x[3], b1 = x[2] - x[5], b2 = x[4] - x[1];
    dft3<INV>(a0, a1, a2); dft3<INV>(b0, b1, b2);
    x[0] = a0; x[4] = a1; x[2] = a2; x[3] = b0; x[1] = b1; x[5] = b2;
}
template <bool INV> DI void dft8(cf (&x)[8]) {
    cf e0 = x[0], e1 = x[2], e2 = x[4], e3 = x[6], o0 = x[1], o1 = x[3], o2 = x[5], o3 = x[7];
    dft4<INV>(e0, e1, e2, e3); dft4<INV>(o0, o1, o2, o3);
    const float h = 0.7071067811865476f;
    const cf t1 = (o1 + muli<INV>(o1)) * h, t2 = muli<INV>(o2), t3 = (muli<INV>(o3) - o3) * h;
    x[0] = e0 + o0; x[4] = e0 - o0; x[1] = e1 + t1; x[5] = e1 - t1;
    x[2] = e2 + t2; x[6] = e2 - t2; x[3] = e3 + t3; x[7] = e3 - t3;
}
DI cf ldc(unsigned a) { return *(const LAS cf*)a; }
DI void stc(unsigned a, cf v) { *(LAS cf*)a = v; }

template <int N> DI void cpow(cf w, cf (&p)[N]) {
    p[0] = w;
    if (N > 1) p[1] = cmul(w, w);
    if (N > 2) p[2] = cmul(p[1], w);
    if (N > 3) p[3] = cmul(p[1], p[1]);
    if (N > 4) p[4] = cmul(p[3], w);
    if (N > 5) p[5] = cmul(p[2], p[2]);
    if (N > 6) p[6] = cmul(p[3], p[2]);
}
DI void tst(__amdgpu_buffer_rsrc_t T, unsigned vo, unsigned so, cf v) { __builtin_amdgcn_raw_buffer_store_b64(__builtin_bit_cast(u32x2, v), T, vo, so, 16); }
DI cf tld(__amdgpu_buffer_rsrc_t T, unsigned vo, unsigned so) { return __builtin_bit_cast(cf, __builtin_amdgcn_raw_buffer_load_b64(T, vo, so, 16)); }
DI size_t tidx(int ka, int b, int k) { return (size_t)(ka * 256 + b) * TP + k; }
struct B1In { unsigned xin[6]; cf w6b, w8b, wk, wb1; };
DI void b1_load(B1In& in, int b, const char* __restrict__ xb) {
    const int tid = threadIdx.x;
    const int a6 = tid / 48, i6 = tid - a6 * 48, k6 = i6 % 6;
    const int t8 = tid < 288 ? tid : 0, a8 = t8 / 36, k8 = t8 - a8 * 36;
    const unsigned* src = (const unsigned*)(xb + (size_t)(b + 256 * a6) * 1152);
#pragma unroll
    for (int n = 0; n < 6; ++n) in.xin[n] = src[i6 + 48 * n];
    in.w6b = tw576(16 * k6); in.w8b = tw576(2 * k8); in.wk = tw576(tid < 289 ? tid : 0);
    int bv = b; asm volatile("" : "+v"(bv)); in.wb1 = tw2048(bv);
}
template <class HOOK>
DI void b1_run(B1In& in, int b, __amdgpu_buffer_rsrc_t T, unsigned A, unsigned B, HOOK hook) {
    const int tid = threadIdx.x;
    const int t6 = tid < 384 ? tid : 0, a6 = t6 / 48, i6 = t6 - a6 * 48, k6 = i6 % 6;
    const int t8 = tid < 288 ? tid : 0, a8 = t8 / 36, k8 = t8 - a8 * 36;
    const unsigned (&xin)[6] = in.xin; const cf w6b = in.w6b, w8b = in.w8b, wk = in.wk;
    if (tid < 384) {
        cf v[6];
#pragma unroll
        for (int n = 0; n < 6; ++n) v[n] = cf{__uint_as_float(xin[n] << 16), __uint_as_float(xin[n] & 0xffff0000u)};
        dft6<false>(v);
#pragma unroll
        for (int m = 0; m < 6; ++m) stc(A + (a6 * 288 + i6 * 6 + m) * 8, v[m]);
    }
    hook(0);
    LBAR();
    if (tid < 384) {
        cf v[6], w6[5]; cpow<5>(w6b, w6);
#pragma unroll
        for (int n = 0; n < 6; ++n) { v[n] = ldc(A + (a6 * 288 + i6 + 48 * n) * 8); if (n) v[n] = cmul(v[n], w6[n - 1]); }
        dft6<false>(v);
#pragma unroll
        for (int m = 0; m < 6; ++m) stc(B + (a6 * 288 + (i6 / 6) * 36 + k6 + 6 * m) * 8, v[m]);
    }
    hook(1);
    LBAR();
    if (tid < 288) {
        cf v[8], w8[7]; cpow<7>(w8b, w8);
#pragma unroll
        for (int n = 0; n < 8; ++n) { v[n] = ldc(B + (a8 * 288 + k8 + 36 * n) * 8); if (n) v[n] = cmul(v[n], w8[n - 1]); }
        dft8<false>(v);
#pragma unroll
        for (int m = 0; m < 8; ++m) stc(A + (a8 * 288 + k8 + 36 * m) * 8, v[m]);
    }
    hook(2);
    LBAR();
    if (tid < 289) {
        const int k = tid, k1 = k == 288 ? 0 : k, k2 = k == 0 ? 0 : 288 - k;
        cf v[8];
#pragma unroll
        for (int a = 0; a < 8; ++a) {
            const cf z1 = ldc(A + (a * 288 + k1) * 8), z2 = ldc(A + (a * 288 + k2) * 8);
            const cf E = cf{z1[0] + z2[0], z1[1] - z2[1]};
            const cf Dd = cf{z1[0] - z2[0], z1[1] + z2[1]};
            const cf O = cf{Dd[1], -Dd[0]};
            v[a] = E + cmul(O, wk);
        }
        dft8<false>(v);
        cf wb[7]; cpow<7>(in.wb1, wb);
#pragma unroll
        for (int ka = 0; ka < 8; ++ka) tst(T, (b * TP + k) * 8, ka * 256 * TP * 8, ka ? cmul(v[ka], wb[ka - 1]) : v[ka]);
    }
    hook(3);
}
struct B2In { cf v[8]; cf w8b, w4b[2]; };
DI void b2_load(B2In& in, int item, __amdgpu_buffer_rsrc_t T) {
    const int t = threadIdx.x, i = t / 10, col = t - i * 10, cb = item >> 3, c = 9 * cb + ((col < 9 || cb == 31) ? col : 0);
#pragma unroll
    for (int n = 0; n < 8; ++n) in.v[n] = tld(T, (i * TP + c) * 8, ((item & 7) * 256 + 32 * n) * TP * 8);
    in.w8b = tw2048(32 * (i & 7)); in.w4b[0] = tw2048(8 * i); in.w4b[1] = tw2048(8 * i + 256);
}
template <class HOOK>
DI void b2_run(B2In& in, int item, __amdgpu_buffer_rsrc_t T, unsigned lbA, unsigned lbB, unsigned lbF, HOOK hook) {
    const int tid = threadIdx.x, cb = item >> 3;
    const int i = tid / 10, col = tid - i * 10;
    const bool act = tid < 320 && (col < 9 || cb == 31);
    const unsigned A = lbA + col * 8, B = lbB + col * 8;
    cf (&v)[8] = in.v;
    const int k = i & 7;
    cf w8[7], w4[2][3];
    if (act) {
        dft8<false>(v);
#pragma unroll
        for (int m = 0; m < 8; ++m) stc(A + (9 * i + m) * 80, v[m]);
        cpow<7>(in.w8b, w8); cpow<3>(in.w4b[0], w4[0]); cpow<3>(in.w4b[1], w4[1]);
    }
    hook(0);
    LBAR();
    if (act) {
#pragma unroll
        for (int n = 0; n < 8; ++n) { v[n] = ldc(A + (i + (i >> 3) + 36 * n) * 80); if (n) v[n] = cmul(v[n], w8[n - 1]); }
        dft8<false>(v);
#pragma unroll
        for (int m = 0; m < 8; ++m) stc(B + ((i >> 3) * 64 + k + 8 * m) * 80, v[m]);
    }
    hook(1);
    LBAR();
    if (act) {
#pragma unroll
        for (int u = 0; u < 2; ++u) {
            cf y[4];
#pragma unroll
            for (int n = 0; n < 4; ++n) { y[n] = ldc(B + (i + 32 * u + 64 * n) * 80); if (n) y[n] = cmul(y[n], w4[u][n - 1]); }
            dft4<false>(y[0], y[1], y[2], y[3]);
#pragma unroll
            for (int m = 0; m < 4; ++m) v[u + 2 * m] = y[m];
        }
        const float sc = 1.f / (4.f * 2048.f * 288.f);
#pragma unroll
        for (int n = 0; n < 8; ++n) {
            const int kb = i + 32 * n;
            const unsigned fa = lbF + kb * 48 + 2 * (col + (((item & 7) + 8 * kb + 9 * cb) & 1));
            const unsigned fr = *(const LAS unsigned short*)fa, fi = *(const LAS unsigned short*)(fa + 24);
            v[n] = cmul(v[n], cf{__uint_as_float(fr << 16) * sc, __uint_as_float(fi << 16) * sc});
        }
        dft8<true>(v);
#pragma unroll
        for (int m = 0; m < 8; ++m) stc(A + (9 * i + m) * 80, v[m]);
    }
    hook(2);
    LBAR();
    if (act) {
#pragma unroll
        for (int n = 0; n < 8; ++n) { v[n] = ldc(A + (i + (i >> 3) + 36 * n) * 80); if (n) v[n] = cmulc(v[n], w8[n - 1]); }
        dft8<true>(v);
#pragma unroll
        for (int m = 0; m < 8; ++m) stc(B + ((i >> 3) * 64 + k + 8 * m) * 80, v[m]);
    }
    hook(3);
    LBAR();
    if (act) {
#pragma unroll
        for (int u = 0; u < 2; ++u) {
            cf y[4];
#pragma unroll
            for (int n = 0; n < 4; ++n) { y[n] = ldc(B + (i + 32 * u + 64 * n) * 80); if (n) y[n] = cmulc(y[n], w4[u][n - 1]); }
            dft4<true>(y[0], y[1], y[2], y[3]);
#pragma unroll
            for (int m = 0; m < 4; ++m) tst(T, (i * TP + 9 * cb + col) * 8, ((item & 7) * 256 + 32 * (u + 2 * m)) * TP * 8, y[m]);
        }
    }
    hook(4);
}
struct B3In { cf v0[8]; cf wpb, w6b, w8b, wb1; };
DI void b3_load(B3In& in, int b, __amdgpu_buffer_rsrc_t U) {
    const int tid = threadIdx.x;
    if (tid < 384) {
        const int a6 = tid / 48, i6 = tid - a6 * 48, k6 = i6 % 6;
        const int t8 = tid < 288 ? tid : 0, a8 = t8 / 36, k8 = t8 - a8 * 36;
#pragma unroll
        for (int ka = 0; ka < 8; ++ka) in.v0[ka] = tld(U, (b * TP + (tid < 289 ? tid : 0)) * 8, ka * 256 * TP * 8);
        in.wpb = tw576(i6); in.w6b = tw576(16 * k6); in.w8b = tw576(2 * k8);
        int bv = b; asm volatile("" : "+v"(bv)); in.wb1 = tw2048(bv);
    }
}
DI void b3_part1(B3In& in, int b, unsigned lb) {
    const int tid = threadIdx.x;
    const unsigned A = lb, B = lb + 8 * 290 * 8;
    if (tid < 289) {
        const bool edge = (unsigned)(tid - 1) >= 287u;
        cf wb[7]; cpow<7>(in.wb1, wb);
#pragma unroll
        for (int ka = 1; ka < 8; ++ka) in.v0[ka] = cmulc(in.v0[ka], wb[ka - 1]);
        dft8<true>(in.v0);
#pragma unroll
        for (int a = 0; a < 8; ++a) {
            const float im = edge ? 0.f : in.v0[a][1];
            stc(A + (a * 290 + tid) * 8, cf{in.v0[a][0], im});
        }
    }
    LBAR();
    if (tid < 384) {
        const int a6 = tid / 48, i6 = tid - a6 * 48;
        const cf c12[6] = {cf{1.f, 0.f}, cf{0.8660254037844386f, -0.5f}, cf{0.5f, -0.8660254037844386f}, cf{0.f, -1.f}, cf{-0.5f, -0.8660254037844386f}, cf{-0.8660254037844386f, -0.5f}};
        cf v[6];
#pragma unroll
        for (int n = 0; n < 6; ++n) {
            const int k = i6 + 48 * n;
            const cf y1 = ldc(A + (a6 * 290 + k) * 8), y2 = ldc(A + (a6 * 290 + 288 - k) * 8);
            const cf E = cf{y1[0] + y2[0], y1[1] - y2[1]};
            const cf O = cmulc(cf{y1[0] - y2[0], y1[1] + y2[1]}, n ? cmul(in.wpb, c12[n]) : in.wpb);
            v[n] = cf{E[0] - O[1], E[1] + O[0]};
        }
        dft6<true>(v);
#pragma unroll
        for (int m = 0; m < 6; ++m) stc(B + (a6 * 288 + i6 * 6 + m) * 8, v[m]);
    }
}
DI void b3_part2(B3In& in, int b, char* __restrict__ out, unsigned lb) {
    const int tid = threadIdx.x;
    const unsigned A = lb, B = lb + 8 * 290 * 8;
    if (tid < 384) {
        const int a6 = tid / 48, i6 = tid - a6 * 48, k6 = i6 % 6;
        cf v[6], w6[5]; cpow<5>(in.w6b, w6);
#pragma unroll
        for (int n = 0; n < 6; ++n) { v[n] = ldc(B + (a6 * 288 + i6 + 48 * n) * 8); if (n) v[n] = cmulc(v[n], w6[n - 1]); }
        dft6<true>(v);
#pragma unroll
        for (int m = 0; m < 6; ++m) stc(A + (a6 * 290 + (i6 / 6) * 36 + k6 + 6 * m) * 8, v[m]);
    }
    LBAR();
    if (tid < 288) {
        const int a8 = tid / 36, k8 = tid - a8 * 36;
        cf v[8], w8[7]; cpow<7>(in.w8b, w8);
#pragma unroll
        for (int n = 0; n < 8; ++n) { v[n] = ldc(A + (a8 * 290 + k8 + 36 * n) * 8); if (n) v[n] = cmulc(v[n], w8[n - 1]); }
        dft8<true>(v);
        unsigned* dst = (unsigned*)(out + ((size_t)(b + 256 * a8) * OUTW + 576) * 2);
#pragma unroll
        for (int m = 0; m < 8; ++m) dst[k8 + 36 * m] = cvtpk(v[m][0], v[m][1]);
    }
}

constexpr float QSCALE = 0.08838834764831845f * 1.4426950408889634f;
constexpr float THR = 8.f;
constexpr unsigned SPIN_MAX = 1u << 17;
#define ACQ "wavefront"
constexpr unsigned QSTASH = 131072;

#define CAPB 16
#define CAPH 40
#define CAPA 14
struct AttnState { f32x16 o[4]; f32x16 negm; float m, l, sa, sb; };
DI bf16x8 frd(unsigned a) { return lds_rd128(a); }
struct DmaPlan { const char* base; unsigned oa, ob; unsigned da, db; unsigned long long ea, eb; };
#define PDV 4
#define SPRIO(x) __builtin_amdgcn_s_setprio(x)
constexpr int PD = PDV;
DI unsigned kfo(int i) { return ((i & 1) * 8 + (i >> 1)) * 1024; }
DI unsigned vfo(int i) { return ((i & 3) * 4 + (i >> 2)) * 1024; }

#define PIN(x) asm volatile("" : "+v"(x))
DI float max3_s(float a, float b, float c) { return __builtin_fmaxf(__builtin_fmaxf(a, b), c); }
DI void dma_set(unsigned lds_addr) { asm volatile("s_mov_b32 m0, %0" :: "s"(lds_addr) : "memory"); }
template <int OFF> DI void dma1(const void* sbase, unsigned voff, unsigned long long en) {
    asm volatile("s_mov_b64 exec, %2\n\tglobal_load_lds_dwordx4 %0, %1 offset:%3 sc1\n\ts_mov_b64 exec, -1" :: "v"(voff), "s"(sbase), "s"(en), "i"(OFF) : "memory");
}
struct Tile { f32x16 s0, s1; unsigned pw[16]; };
struct OpTab { int n; int type[80]; int idx[80]; int slot[80]; };
constexpr OpTab make_ops() {
    OpTab o{}; int n = 0;
    for (int j = 0; j <= 16; ++j) {
        if (j < 16) { o.type[n] = 0; o.idx[n++] = 2 * j; }
        if (j > 0) { o.type[n] = 1; o.idx[n++] = 2 * j - 2; }
        if (j < 16) { o.type[n] = 0; o.idx[n++] = 2 * j + 1; }
        if (j > 0) { o.type[n] = 1; o.idx[n++] = 2 * j - 1; o.type[n] = 2; o.idx[n++] = j - 1; }
    }
    o.n = n;
    int total = 0; for (int i = 0; i < n; ++i) total += o.type[i] == 0 ? 8 : 4;
    const int capsum = 11 * CAPB + CAPH + 16 * CAPA;
    int cum = 0;
    for (int i = 0; i < n; ++i) {
        const int cost = o.type[i] == 0 ? 8 : 4;
        const long mid = (long)(2 * cum + cost) * capsum / (2 * total);
        int slot = 0, edge = 0;
        for (; slot < 27; ++slot) { edge += slot < 11 ? CAPB : slot == 11 ? CAPH : CAPA; if (mid < edge) break; }
        o.slot[i] = slot; cum += cost;
    }
    return o;
}
constexpr OpTab OPS = make_ops();
template <int I> DI void run_op(Tile& T, float& sa, float& sb) {
    constexpr int ty = OPS.type[I], e = OPS.idx[I];
    if constexpr (ty == 0) {
        if constexpr (e < 16) { float v = __builtin_amdgcn_exp2f(T.s0[e]); PIN(v); T.s0[e] = v; } else { float v = __builtin_amdgcn_exp2f(T.s1[e - 16]); PIN(v); T.s1[e - 16] = v; }
    } else if constexpr (ty == 1) {
        const float v = e < 16 ? T.s0[e & 15] : T.s1[e & 15];
        if constexpr (e & 1) { sb += v; PIN(sb); } else { sa += v; PIN(sa); }
    } else {
        const float lo = e < 8 ? T.s0[(2 * e) & 15] : T.s1[(2 * e) & 15], hi = e < 8 ? T.s0[(2 * e + 1) & 15] : T.s1[(2 * e + 1) & 15];
        unsigned w = cvtpk(lo, hi); PIN(w); T.pw[e] = w;
    }
}
template <int S, int I = 0> DI void run_slot(Tile& T, float& sa, float& sb) {
    if constexpr (I < OPS.n) {
        if constexpr (OPS.slot[I] == S) run_op<I>(T, sa, sb);
        run_slot<S, I + 1>(T, sa, sb);
    }
}
DI bf16x8 pfrag(const Tile& T, int ks) { u32x4 w = {T.pw[4 * ks], T.pw[4 * ks + 1], T.pw[4 * ks + 2], T.pw[4 * ks + 3]}; return __builtin_bit_cast(bf16x8, w); }
DI f32x16 mfma_initc(bf16x8 a, bf16x8 b, const f32x16& c) {
    f32x16 d; asm("v_mfma_f32_32x32x16_bf16 %0, %1, %2, %3" : "=&v"(d) : "v"(a), "v"(b), "v"(c)); return d;
}

template <int I> DI void dma_gap(const DmaPlan& dp, unsigned l16) {
    const unsigned va = l16 + dp.oa, vb = l16 + dp.ob;
    if (I == 0) dma_set(dp.da);
    if (I == 1) dma1<0>(dp.base, va, dp.ea);
    if (I == 3) dma1<1024>(dp.base, va, dp.ea);
    if (I == 5) dma1<2048>(dp.base, va, dp.ea);
    if (I == 7) dma1<3072>(dp.base, va, dp.ea);
    if (I == 8) dma_set(dp.db);
    if (I == 9) dma1<0>(dp.base, vb, dp.eb);
    if (I == 11) dma1<1024>(dp.base, vb, dp.eb);
    if (I == 13) dma1<2048>(dp.base, vb, dp.eb);
    if (I == 15) dma1<3072>(dp.base, vb, dp.eb);
}
template <int I, bool LEAD, class NEXT> DI void side_gap(const DmaPlan& dp, unsigned l16, NEXT& next) {
    if (LEAD) { dma_gap<I>(dp, l16); if (I == 7) SPRIO(2); if (I == 15) SPRIO(1); }
    else { next(I); if (I == 7) SPRIO(0); }
}
template <bool QK, bool FIN, bool LEAD, bool PRE, bool NXT, class NEXT>
DI void phaseA(AttnState& st, const bf16x8 (&qf)[8], bf16x8 (&pre)[PD], Tile& C, Tile& P, unsigned kaddr, unsigned vaddr, const DmaPlan dp,
               unsigned l16, NEXT next) {
    bf16x8 fr[16 + PD];
    if (QK) {
#pragma unroll
        for (int i = 0; i < PD; ++i) fr[i] = PRE ? pre[i] : frd(kaddr + kfo(i));
    } else if (NXT) {
#pragma unroll
        for (int i = 0; i < PD; ++i) fr[16 + i] = frd(vaddr + vfo(i));
    }
    if (LEAD) SPRIO(3);
    SB();
    if (FIN) { run_slot<11>(P, st.sa, st.sb); SB(); }
#define GA(I) do { \
        if (QK) { \
            if ((I) + PD < 16) fr[(I) + PD] = frd(kaddr + kfo((I) + PD)); else if (NXT) fr[(I) + PD] = frd(vaddr + vfo((I) + PD - 16)); \
            if ((I) == 0) C.s0 = mfma_initc(fr[I], qf[0], st.negm); else if ((I) == 1) C.s1 = mfma_initc(fr[I], qf[0], st.negm); \
            else if ((I) & 1) C.s1 = MFMA32(fr[I], qf[(I) >> 1], C.s1); else C.s0 = MFMA32(fr[I], qf[(I) >> 1], C.s0); \
        } \
        if (QK && (I) == 4) asm volatile("" :: "v"(st.negm));        \
        if (FIN) run_slot<12 + (I)>(P, st.sa, st.sb); \
        side_gap<I, LEAD>(dp, l16, next); \
        SB(); } while (0)
    GA(0); GA(1); GA(2); GA(3); GA(4); GA(5); GA(6); GA(7); GA(8); GA(9); GA(10); GA(11); GA(12); GA(13); GA(14); GA(15);
#undef GA
    if (NXT) {
#pragma unroll
        for (int i = 0; i < PD; ++i) pre[i] = fr[16 + i];
    }
}
template <bool QK, bool FIN, bool LEAD, bool NXT, class FLUSH, class NEXT>
DI void phaseB(AttnState& st, bf16x8 (&pre)[PD], Tile& C, Tile& P, unsigned vaddr, unsigned kaddr, int fm, bool needmask, int dq,
               const DmaPlan dp, unsigned l16, FLUSH flush, NEXT next) {
    bf16x8 vr[16 + PD];
    if (FIN) {
#pragma unroll
        for (int i = 0; i < PD; ++i) vr[i] = pre[i];
    }
    if (LEAD) SPRIO(3);
#define PV1(I) do { \
        if ((I) + PD < 16) { if (FIN) vr[(I) + PD] = frd(vaddr + vfo((I) + PD)); } else if (NXT) vr[(I) + PD] = frd(kaddr + kfo((I) + PD - 16)); \
        if (FIN) st.o[(I) & 3] = MFMA32(vr[I], pfrag(P, (I) >> 2), st.o[(I) & 3]); \
        side_gap<I, LEAD>(dp, l16, next); } while (0)
    PV1(0);
    if (FIN) { st.l += st.sa + st.sb; st.sa = 0.f; st.sb = 0.f; }
    SB();
    PV1(1); SB();
    float f = 1.f, lold = 0.f; bool resc = false;
    if (QK) {
        if (needmask) {
            const float NEG = -__builtin_inff();
#pragma unroll
            for (int i = 0; i < 16; ++i) {
                const int c = (i & 3) + 8 * (i >> 2);
                if (c > dq) C.s0[i] = NEG;
                if (c + 32 > dq) C.s1[i] = NEG;
            }
        }
        SB();
        float ma = max3_s(C.s0[0], C.s0[1], C.s0[2]), mb = max3_s(C.s0[3], C.s0[4], C.s0[5]);
        ma = max3_s(ma, C.s0[6], C.s0[7]); mb = max3_s(mb, C.s0[8], C.s0[9]); ma = max3_s(ma, C.s0[10], C.s0[11]);
        PIN(ma); PIN(mb);
        PV1(2); SB();
        mb = max3_s(mb, C.s0[12], C.s0[13]); ma = max3_s(ma, C.s0[14], C.s0[15]); mb = max3_s(mb, C.s1[0], C.s1[1]);
        ma = max3_s(ma, C.s1[2], C.s1[3]); mb = max3_s(mb, C.s1[4], C.s1[5]);
        PIN(ma); PIN(mb);
        PV1(3); SB();
        ma = max3_s(ma, C.s1[6], C.s1[7]); mb = max3_s(mb, C.s1[8], C.s1[9]); ma = max3_s(ma, C.s1[10], C.s1[11]);
        mb = max3_s(mb, C.s1[12], C.s1[13]); ma = max3_s(ma, C.s1[14], C.s1[15]);
        float mx = __builtin_fmaxf(ma, mb);
        PIN(mx);
        PV1(4); SB();
        const float thr = __int_as_float(0x41000000 + (fm & (int)(0xff800000u - 0x41000000u)));
        if (__builtin_expect(__any(mx > thr), 0)) {
            mx = fmaxf(mx, swap32f(mx));
            const float dl = fmaxf(mx, __int_as_float(fm & (int)0xff800000u));
            f = __builtin_amdgcn_exp2f(-fmaxf(dl, 0.f)) * __int_as_float(~fm & 0x3f800000);
            st.m += dl; lold = st.l; st.l *= f; resc = true;
#pragma unroll
            for (int i = 0; i < 16; ++i) { C.s0[i] -= dl; C.s1[i] -= dl; st.negm[i] = -st.m; }
        }
        SB();
    } else {
        PV1(2); SB(); PV1(3); SB(); PV1(4); SB();
    }
#define GB(i) do { PV1(i); if (QK) run_slot<(i) - 5>(C, st.sa, st.sb); SB(); } while (0)
    GB(5); GB(6); GB(7); GB(8); GB(9); GB(10); GB(11); GB(12); GB(13); GB(14); GB(15);
#undef GB
#undef PV1
    if (__builtin_expect(resc, 0)) {
        flush(lold);
#pragma unroll
        for (int d = 0; d < 4; ++d)
#pragma unroll
            for (int i = 0; i < 16; ++i) st.o[d][i] *= f;
    }
    if (NXT) {
#pragma unroll
        for (int i = 0; i < PD; ++i) pre[i] = vr[16 + i];
    }
}
DI void cst(__amdgpu_buffer_rsrc_t cr, unsigned vo, bf16x8 v) { __builtin_amdgcn_raw_buffer_store_b128(__builtin_bit_cast(u32x4, v), cr, vo, 0, 16); }
DI void store_ctx(const f32x16 (&o)[4], float rl, __amdgpu_buffer_rsrc_t cr, int row0, int h, int lane) {
    const int r = lane & 31, hh = lane >> 5;
    const unsigned base = (((row0 >> 4) + (r >> 4)) * 128 + h * 4) * 1024 + ((r & 15) + 16 * hh) * 16;
#pragma unroll
    for (int db = 0; db < 4; ++db) {
        f32x16 t;
#pragma unroll
        for (int i = 0; i < 16; ++i) t[i] = o[db][i] * rl;
        cst(cr, base + db * 1024, pack8<0>(t));
        cst(cr, base + db * 1024 + 512, pack8<1>(t));
    }
}

__global__ __launch_bounds__(512, 2) void k_main(const char* __restrict__ x, const char* __restrict__ wq, const char* __restrict__ c,
                                                  const char* __restrict__ wuk, const char* __restrict__ wuv, char* ws,
                                                  const char* __restrict__ wr, const char* __restrict__ wi_, const char* __restrict__ wo,
                                                  const char* __restrict__ xb, char* __restrict__ out) {
    extern __shared__ __attribute__((aligned(16))) char smem[];
    const unsigned lds0 = (unsigned)(uintptr_t)smem;
    const int tid = threadIdx.x, lane = tid & 63, r = lane & 31, hh = lane >> 5;
    const int w = rfl(tid >> 6);
    const int b = blockIdx.x, h = 4 * (b & 7) + ((b >> 3) & 3), j = b >> 5;
    const int grp = w >> 2, wi = w & 3;
    const int rowL = 128 * j + 32 * wi, rowH = 128 * (15 - j) + 32 * wi;
    const char* kvh = ws + WS_KV + (size_t)h * 32 * 32768;
    char* flg = ws + WS_FLAG;
    u64 ep = __hip_atomic_load((u64*)(flg + FL_E), __ATOMIC_RELAXED, __HIP_MEMORY_SCOPE_AGENT);
    const unsigned l16 = lane * 16;
    const char* pc0; asm volatile("s_getpc_b64 %0" : "=s"(pc0));
    auto pfcode = [&](unsigned lo, unsigned hi) {
        const unsigned per = ((hi - lo) / 128 + 31) / 32, ln = (b >> 3) * per + lane;
        const unsigned off = lo + min(lane < (int)per ? ln : 0u, (hi - lo) / 128 - 1) * 128;
        asm volatile("global_load_ushort v255, %0, %1" :: "v"(off), "s"(pc0) : "memory", "v255");
    };
#define CODE_BYTES 66560
    if (w == 0) pfcode(0, CODE_BYTES);
    const unsigned qst = lds0 + QSTASH + wi * 8192 + l16;
    const __amdgpu_buffer_rsrc_t Tr = __builtin_amdgcn_make_buffer_rsrc((void*)(ws + WS_T), 0, 8 * 256 * TP * 8, 0x00020000);
    const __amdgpu_buffer_rsrc_t cr = __builtin_amdgcn_make_buffer_rsrc((void*)(ws + WS_CTX), 0, 16 * 1024 * 1024, 0x00020000);
#define FSET(off) __hip_atomic_store((u64*)(flg + (off)), ep, __ATOMIC_RELAXED, __HIP_MEMORY_SCOPE_AGENT)
#define FGET(off) __hip_atomic_load((u64*)(flg + (off)), __ATOMIC_RELAXED, __HIP_MEMORY_SCOPE_AGENT)
    unsigned aoc[2], aox[2];
#pragma unroll
    for (int i = 0; i < 2; ++i) { const int row = 8 * i + (lane >> 3), ch = ((lane & 7) ^ ((row >> 1) & 7)) * 16; aoc[i] = row * 1024 + ch; aox[i] = row * 1152 + ch; }
    const char* cbase = c + (size_t)(j * 256 + 32 * w) * 1024;
    const char* xbase = x + (size_t)(grp ? rowH : rowL) * 1152;
    const unsigned woff = (lane >> 4) * 8192 + ((((lane >> 2) & 3) ^ ((lane >> 4) & 3)) * 64) + (lane & 3) * 16;
    const char* wkvbase = (w < 4 ? wuk : wuv) + h * 256 + (size_t)(8 * wi) * 8192;
    const char* wqbase = wq + h * 256 + (size_t)(4 * w) * 8192;
    unsigned w1k = w * 1024; asm volatile("" : "+s"(w1k));
    auto issA = [&](int m, unsigned d, int p, bool isx) {
        if (isx) glds(xbase + (m - 8) * 128 + (p >> 1) * 16 * 1152, aox[p & 1], d + 4 * w1k + p * 1024);
        else glds(cbase + m * 128 + (p >> 1) * 16384, aoc[p & 1], d + 4 * w1k + p * 1024);
    };
    auto issW = [&](int g, int p, bool isq) {
        const unsigned d = lds0 + WRING + (g & 3) * 16384;
        if (isq) { if (p == 0) glds(wqbase + (size_t)(g - 16) * 32 * 8192, woff, d + w1k); }
        else glds(wkvbase + (size_t)g * 32 * 8192 + p * 32768, woff, d + 2 * w1k + p * 1024);
    };
    auto iss = [&](int g, unsigned fa_, int i, int dm) {
        if (i < 2) issA((g >> 1) + 2, fa_, 2 * (g & 1) + i, dm >= 2);
        else issW(g + 3, i - 2, dm == 3);
    };
#pragma unroll
    for (int p = 0; p < 4; ++p) issA(0, lds0, p, false);
    issW(0, 0, false); issW(0, 1, false); issW(1, 0, false); issW(1, 1, false);
#pragma unroll
    for (int p = 0; p < 4; ++p) issA(1, lds0 + 32768, p, false);
    issW(2, 0, false); issW(2, 1, false);
    const int g4 = lane >> 4, q4 = (lane & 15) >> 2, p4 = lane & 3;
    bf16x8 qf[8];
    {
        const int kh = w & 1, dh = (w >> 1) & 1;
        unsigned cx[4], xx[4];
#pragma unroll
        for (int s = 0; s < 4; ++s) { const unsigned ch = ((2 * s + hh) ^ ((r >> 1) & 7)) * 16; cx[s] = (128 * kh + r) * 128 + ch; xx[s] = (32 * w + r) * 128 + ch; }
        const unsigned wxx = (grp ? 8192 : 0) + (8 * hh + q4) * 256 + 32 * (g4 & 1) + 8 * p4;
        const unsigned wx0 = wxx + (((2 * dh) ^ q4) * 64), wx1 = wxx + (((2 * dh + 1) ^ q4) * 64);
        const __amdgpu_buffer_rsrc_t kvr = __builtin_amdgcn_make_buffer_rsrc((void*)kvh, 0, 32 * 32768, 0x00020000);
        const unsigned ovoff = (j * 4 + kh * 2) * 32768 + dh * (grp ? 8192 : 4096) + l16;
        unsigned wx[4];
#pragma unroll
        for (int db = 0; db < 4; ++db) wx[db] = (8 * hh + q4) * 256 + 32 * (g4 & 1) + 8 * p4 + ((db ^ q4) * 64);
        f32x16 acc[4];
        int as = 2;
        bf16x8 xa, xb2; s16x4 wa[8], wb[8];
#define QST(i) case i: kv_store1<i>(pk, kvr, ovoff, grp); break;
#define QH(WF, XF, WN, XN, WADDR, XADDR, G_, DM, ST) do { \
            _Pragma("unroll") for (int db = 0; db < 4; ++db) { \
                WN[2 * db] = lds_tr((WADDR) + wx[db]); WN[2 * db + 1] = lds_tr((WADDR) + wx[db] + 1024); \
                if (db == 0) XN = lds_rd128(XADDR); \
                acc[db] = MFMA32(__builtin_shufflevector(WF[2 * db], WF[2 * db + 1], 0, 1, 2, 3, 4, 5, 6, 7), XF, acc[db]); \
                if ((ST) && db == 0) { switch ((G_) - 16) { QST(0) QST(1) QST(2) QST(3) QST(4) QST(5) QST(6) QST(7) QST(8) QST(9) QST(10) QST(11) QST(12) QST(13) QST(14) QST(15) default: break; } } \
                if (((DM) & 1) && db >= 1 && db < 3) iss(G_, fa_, db - 1, 3); \
                if (((DM) & 2) && db == 3) iss(G_, fa_, 2, 3); \
                SB(); } } while (0)
#define QTILE(BAR0, BAR1, DM0, DM1, ST) do { \
            const unsigned ab = lds0 + as * 32768, an = lds0 + (as == 2 ? 0 : as + 1) * 32768, fa_ = lds0 + (as == 0 ? 2 : as - 1) * 32768; \
            const unsigned wA = lds0 + WRING + ((2 * m) & 3) * 16384, wB = wA + 16384, wC = lds0 + WRING + ((2 * m + 2) & 3) * 16384; \
            BAR0; SB(); \
            QH(wa, xa, wb, xb2, wA + 4096, ab + xx[1], 2 * m, DM0, ST); \
            QH(wb, xb2, wa, xa, wB, ab + xx[2], 0, 0, false); \
            BAR1; SB(); \
            QH(wa, xa, wb, xb2, wB + 4096, ab + xx[3], 2 * m + 1, DM1, ST); \
            QH(wb, xb2, wa, xa, wC, an + xx[0], 0, 0, false); \
            as = as == 2 ? 0 : as + 1; } while (0)
#define BAR_ONLY() asm volatile("s_barrier" ::: "memory")
        u32x4 pk[16];
        const char* pfx = x + (size_t)(b >> 3) * 73728 + w1k;
        const char* pfw = wq + (size_t)((b >> 3) * 18 + w) * 8192 + (b & 7) * 1024;
        auto pf = [&](int g, unsigned& d0) {
            const char* p = g < 9 ? pfx + g * 8192 : pfw + (size_t)((g - 9) * 8 - ((g == 11 && w >= 2) ? 8 : 0)) * 8192;
            asm volatile("global_load_ushort v255, %0, %1" :: "v"(l16), "s"(p) : "memory", "v255");
        };
        if (grp) kv_run<true>(lds0, wx0, wx1, cx, iss, pk, pf); else kv_run<false>(lds0, wx0, wx1, cx, iss, pk, pf);
#pragma unroll
        for (int i = 0; i < 4; ++i) acc[i] = f32x16{};
        {   const unsigned sb = lds0 + WRING;
            wa[0] = lds_tr(sb + wx[0]); wa[1] = lds_tr(sb + wx[0] + 1024); xa = lds_rd128(lds0 + 2 * 32768 + xx[0]);
#pragma unroll
            for (int db = 1; db < 4; ++db) { wa[2 * db] = lds_tr(sb + wx[db]); wa[2 * db + 1] = lds_tr(sb + wx[db] + 1024); } }
        { const int m = 8; QTILE(VM_BAR(3), VM_BAR(4), 3, 3, true); }
        for (int m = 9; m < 15; ++m) { QTILE(VM_BAR(4), VM_BAR(4), 3, 3, true); }
        B1In b1in;
        { const int m = 15; QTILE({ VM_BAR(4); issW(33, 0, true); }, { VM_BAR(1); if (w < 6) b1_load(b1in, b, xb); }, 0, 0, true); }
        const int m = 16;
        const unsigned ab = lds0 + as * 32768, fa_ = 0;
        const unsigned wA = lds0 + WRING + ((2 * m) & 3) * 16384, wB = wA + 16384;
        BAR_ONLY();
        SB();
        QH(wa, xa, wb, xb2, wA + 4096, ab + xx[1], 0, 0, false);
        QH(wb, xb2, wa, xa, wB, ab + xx[2], 0, 0, false);
        VM_BAR(0);
        ep = (((u64)rfl((unsigned)(ep >> 32)) << 32) | rfl((unsigned)ep)) + 1;
        if (tid == 0) FSET(FL_KV + (h * 8 + j) * 128);
        SB();
        QH(wa, xa, wb, xb2, wB + 4096, ab + xx[3], 0, 0, false);
        QH(wb, xb2, wa, xa, wB, ab + xx[0], 0, 0, false);
#pragma unroll
        for (int db = 0; db < 4; ++db) {
#pragma unroll
            for (int i = 0; i < 16; ++i) acc[db][i] *= QSCALE;
            qf[2 * db] = pack8<0>(acc[db]); qf[2 * db + 1] = pack8<1>(acc[db]);
        }
        if (grp) {
#pragma unroll
            for (int f = 0; f < 8; ++f) *(LAS bf16x8*)(qst + f * 1024) = qf[f];
        }
        bool got = false; u64 pend = 0;
        auto poll = [&]() {
            if (w == 7 && !got) {
                if (__all(pend == ep)) {
                    got = true; __builtin_amdgcn_fence(__ATOMIC_ACQUIRE, ACQ);
#pragma nounroll
                    for (int i = 0; i < 16; ++i) glds_sc1(kvh + i * 1024, l16, lds0 + 3 * 32768 + i * 1024);
                } else pend = FGET(FL_KV + (h * 8 + (lane & 7)) * 128);
            } };
        pend = ~ep;
        auto hook = [&](int seg) {
            poll();
            if (w == 6) {
                if (seg == 0) {
                    const char* wsrc = wo + (size_t)(32 * (b >> 1)) * 1152 + (b & 1) * 576;
#pragma unroll
                    for (int i = 0; i < 9; ++i) { const int ci = i * 64 + lane, row = ci / 18, cc = ci - row * 18;
                        glds(wsrc, row * 1152 + cc * 16, lds0 + 20480 + i * 1024); glds(wsrc + 288, row * 1152 + cc * 16, lds0 + 86016 + i * 1024); }
                } else if (seg == 3) {
                    asm volatile("s_waitcnt vmcnt(0)" ::: "memory");
                    const unsigned ra = lds0 + (16 * (g4 >> 1) + 4 * (g4 & 1) + q4) * 288 + p4 * 8;
                    const __amdgpu_buffer_rsrc_t wfr = __builtin_amdgcn_make_buffer_rsrc((void*)(ws + WS_WOF), 0, 36 * 128 * 1024, 0x00020000);
#pragma unroll
                    for (int n = 0; n < 18; ++n) { const unsigned a = ra + (n < 9 ? 20480 : 86016) + (n % 9) * 32;
                        cst(wfr, (((b & 1) * 18 + n) * 128 + (b >> 1)) * 1024 + l16, lds_tr2(a, a + 8 * 288)); }
                }
            } };
        b1_run(b1in, b, Tr, lds0, lds0 + 65536, hook);
        if (w == 7) {
            for (unsigned spin = 0; spin < SPIN_MAX && !got; ++spin) { poll(); __builtin_amdgcn_s_sleep(1); }
        }
    }
    const int n1 = 2 * j + 2;
    AttnState st;
#pragma unroll
    for (int d = 0; d < 4; ++d) st.o[d] = f32x16{};
    st.negm = f32x16{}; st.m = 0.f; st.l = 0.f;
    st.sa = 0.f; st.sb = 0.f;
    DmaPlan dp;
    const char* kvw = kvh + wi * 4096;
    const unsigned ldw = lds0 + wi * 4096;
    int szero; asm volatile("s_mov_b32 %0, 0" : "=s"(szero));
    int n_sw, n_fm, n_ps, n_dq = szero, tl_n = szero; unsigned n_ka = lds0 + 3 * 32768, n_va = szero;
    const int mg = -grp;
    dp.base = kvw; dp.oa = szero; dp.da = ldw + 3 * 32768;
    auto plan = [&](int t, int part) {
#define SPIN(x) asm volatile("" : "+s"(x))
        if (part == 2) {
            n_va = n_ka + 16384; SPIN(n_va); n_ka = dp.da - wi * 4096; SPIN(n_ka);
            dp.ob = dp.oa + 16384; SPIN(dp.ob); dp.db = dp.da + 16384; SPIN(dp.db);
        } else if (part == 5) {
            const int m1v = (t - n1) >> 31;
            n_dq = rowH + (~mg & m1v & (rowL - rowH)) - 64 * tl_n; SPIN(n_dq);
            const int ev = ~(m1v & ~mg) & ((t - 17 + szero) >> 31);
            dp.eb = (unsigned long long)(long long)ev; SPIN(dp.eb);
        } else if (part == 8) {
            const int m1k = (t + 1 - n1) >> 31;
            tl_n = t + 1 + (~m1k & (t + 1 - n1 + grp)); SPIN(tl_n);
            const int ek = ~(m1k & mg) & ((t - 16 + szero) >> 31);
            dp.ea = (unsigned long long)(long long)ek; SPIN(dp.ea);
        } else if (part == 11) {
            dp.oa = (unsigned)((tl_n & 31) << 15); SPIN(dp.oa); dp.da = ldw + ((tl_n + 3) & 3) * 32768; SPIN(dp.da);
            n_sw = ~mg & (((t ^ n1) - 1) >> 31); SPIN(n_sw);
            n_fm = n_sw | ((t - 1 + szero) >> 31); SPIN(n_fm);
            n_ps = (t + 1 - n1) | grp; SPIN(n_ps);
        }
    };
    Tile TA, TB;
    const int rl4 = r - 4 * hh;
    bf16x8 pre[PD];
    auto nop = [&](int) {};
    auto nof = [&](float) {};
    plan(0, 2); plan(0, 5); plan(0, 8); plan(0, 11);
#define STEPL(FIN_, C_, P_, t) do { \
        const int sw_ = n_sw, fm_ = n_fm; const bool presw_ = n_ps == 0; \
        WAIT_BAR0(); \
        const unsigned va_ = n_va + l16; const int dq_ = n_dq; \
        phaseA<true, FIN_, true, false, FIN_>(st, qf, pre, C_, P_, n_ka + l16, va_, dp, l16, nop); \
        phaseB<true, FIN_, false, false>(st, pre, C_, P_, va_, 0, fm_, dq_ < 63, dq_ + rl4, dp, l16, \
            [&](float lo_) { if (FIN_ && sw_) { const float lt_ = lo_ + swap32f(lo_); store_ctx(st.o, 1.f / lt_, cr, rowL, h, lane); } }, \
            [&](int part) { plan((t) + 1, part); }); \
        if (presw_) { asm volatile("ds_read_b128 %0, %8\n\tds_read_b128 %1, %8 offset:1024\n\tds_read_b128 %2, %8 offset:2048\n\tds_read_b128 %3, %8 offset:3072\n\t" \
            "ds_read_b128 %4, %8 offset:4096\n\tds_read_b128 %5, %8 offset:5120\n\tds_read_b128 %6, %8 offset:6144\n\tds_read_b128 %7, %8 offset:7168\n\ts_waitcnt lgkmcnt(0)" \
            : "+v"(qf[0]), "+v"(qf[1]), "+v"(qf[2]), "+v"(qf[3]), "+v"(qf[4]), "+v"(qf[5]), "+v"(qf[6]), "+v"(qf[7]) : "v"(qst) : "memory"); } \
        } while (0)
    STEPL(false, TA, TB, 0);
    if (tid == 0) FSET(FL_S + b * 8);
    for (int t = 1; t < 17; t += 2) {
        STEPL(true, TB, TA, t);
        STEPL(true, TA, TB, t + 1);
    }
    u64 pv_[4];
    auto wait_all = [&](size_t off) {
        for (unsigned spin = 0; spin < SPIN_MAX; ++spin) {
            if (__all(pv_[0] == ep && pv_[1] == ep && pv_[2] == ep && pv_[3] == ep)) break;
            if (spin) __builtin_amdgcn_s_sleep(1);
#pragma unroll
            for (int i = 0; i < 4; ++i) pv_[i] = FGET(off + (i * 64 + lane) * 8);
        } };
    if (w == 7) { pv_[0] = pv_[1] = pv_[2] = pv_[3] = ~ep; wait_all(FL_S); __builtin_amdgcn_fence(__ATOMIC_ACQUIRE, ACQ); }
    WAIT_BAR0();
    B2In b2in;
    typedef unsigned u32x3 __attribute__((ext_vector_type(3)));
    u32x3 fw[8];
    {
        unsigned oo[2], oo2[2];
#pragma unroll
        for (int q2 = 0; q2 < 2; ++q2) {
            const int kb = ((w - 5) & 1) * 128 + q2 * 64 + lane;
            oo[q2] = ((((b & 7) + 8 * kb) * 289 + 9 * (b >> 3)) * 2) & ~3u;
            oo2[q2] = min(oo[q2] + 12u, 2048u * 578u - 12u);
            asm volatile("" : "+v"(oo[q2]), "+v"(oo2[q2]));
        }
        if (w < 5) b2_load(b2in, b, Tr);
        asm volatile("" ::: "memory");
        if (w == 5 || w == 6) {
#pragma unroll
            for (int q2 = 0; q2 < 2; ++q2) {
                typedef u32x3 u32x3a __attribute__((aligned(4)));
                fw[4 * q2] = *(const u32x3a*)(wr + oo[q2]); fw[4 * q2 + 1] = *(const u32x3a*)(wr + oo2[q2]);
                fw[4 * q2 + 2] = *(const u32x3a*)(wi_ + oo[q2]); fw[4 * q2 + 3] = *(const u32x3a*)(wi_ + oo2[q2]);
            }
        }
    }
    SB();
    phaseA<false, true, true, false, true>(st, qf, pre, TB, TA, 0, n_va + l16, dp, l16, nop);
    phaseB<false, true, false, false>(st, pre, TB, TA, n_va + l16, 0, 0, false, 0, dp, l16, nof, nop);
    SPRIO(0);
    const int row0 = rowH;
    LBAR();
    {
        const unsigned mb = lds0 + wi * 17408 + grp * 8704, mo = lds0 + wi * 17408 + (1 - grp) * 8704;
#pragma unroll
        for (int d = 0; d < 2; ++d)
#pragma unroll
            for (int i4 = 0; i4 < 4; ++i4) {
                const f32x16& src = grp ? st.o[d] : st.o[2 + d];
                f32x4 v = {src[4 * i4], src[4 * i4 + 1], src[4 * i4 + 2], src[4 * i4 + 3]};
                *(LAS f32x4*)(mb + (d * 4 + i4) * 1024 + l16) = v;
            }
        { f32x2 ml = {st.m, st.l}; *(LAS f32x2*)(mb + 8192 + lane * 8) = ml; }
        LBAR();
        const f32x2 ml = *(const LAS f32x2*)(mo + 8192 + lane * 8);
        const float mn = fmaxf(st.m, ml[0]);
        const float fa = __builtin_amdgcn_exp2f(st.m - mn), fb = __builtin_amdgcn_exp2f(ml[0] - mn);
        float lt = st.l * fa + ml[1] * fb; lt += swap32f(lt);
        const float rl = 1.f / lt, ra = fa * rl, rbb = fb * rl;
        const unsigned base = (((row0 >> 4) + (r >> 4)) * 128 + h * 4 + 2 * grp) * 1024 + ((r & 15) + 16 * hh) * 16;
#pragma unroll
        for (int d = 0; d < 2; ++d) {
            const f32x16& own = grp ? st.o[2 + d] : st.o[d];
            f32x16 t;
#pragma unroll
            for (int i4 = 0; i4 < 4; ++i4) {
                const f32x4 v = *(const LAS f32x4*)(mo + (d * 4 + i4) * 1024 + l16);
#pragma unroll
                for (int e = 0; e < 4; ++e) t[4 * i4 + e] = own[4 * i4 + e] * ra + v[e] * rbb;
            }
            cst(cr, base + d * 1024, pack8<0>(t));
            cst(cr, base + d * 1024 + 512, pack8<1>(t));
        }
    }
    const int xcd = b & 7, ks = xcd >> 1, idx = (b >> 3) + 32 * (xcd & 1), mbk = idx >> 2, nb = idx & 3;
    const int mi = wi, kh = grp;
    const char* abase = ws + WS_CTX + ((size_t)(mbk * 8 + mi * 2) * 128 + ks * 32 + kh * 16) * 1024;
    const char* bbase = ws + WS_WOF + ((size_t)(nb * 9) * 128 + ks * 32) * 1024;
    unsigned boff[5];
#pragma unroll
    for (int i = 0; i < 5; ++i) { const int p = (w + 8 * i) % 36, hf = p / 18, u = (p % 18) / 9, n = p % 9; boff[i] = (n * 128 + hf * 16 + u) * 1024 + l16; }
    const unsigned long long en5 = (unsigned long long)(long long)((w - 4) >> 31);
    bf16x8 af[4][4];
    auto issueA = [&](int s_, bf16x8 (&a)[4]) {
        asm volatile("global_load_dwordx4 %0, %4, %5 sc1\n\tglobal_load_dwordx4 %1, %4, %5 offset:1024 sc1\n\tglobal_load_dwordx4 %2, %4, %6 sc1\n\tglobal_load_dwordx4 %3, %4, %6 offset:1024 sc1"
                     : "=&v"(a[0]), "=&v"(a[1]), "=&v"(a[2]), "=&v"(a[3]) : "v"(l16), "s"(abase + s_ * 2048), "s"(abase + 131072 + s_ * 2048) : "memory");
    };
    auto issueB = [&](int s_) {
        const unsigned d = lds0 + (s_ & 3) * 36864 + w1k;
#pragma unroll
        for (int i = 0; i < 4; ++i) glds_sc1(bbase + s_ * 2048, boff[i], d + i * 8192);
        asm volatile("s_mov_b32 m0, %2\n\ts_mov_b64 exec, %3\n\tglobal_load_lds_dwordx4 %0, %1 sc1\n\ts_mov_b64 exec, -1" :: "v"(boff[4]), "s"(bbase + s_ * 2048), "s"(rfl(d + 32768)), "s"(en5) : "memory");
    };
    {
        const unsigned lbA = lds0 + 73728, lbB = lbA + 23040, lbF = lbB + 20480;
        const int jp = mbk < 8 ? mbk : 15 - mbk;
        bool got = false; u64 pend = ~ep;
        auto poll = [&]() {
            if (w == 7 && !got) { if (__all(pend == ep)) got = true; else pend = FGET(FL_CTX + ((8 * ks + (lane & 7)) * 8 + jp) * 128); } };
        auto hook = [&](int seg) {
            if (seg == 0) {
                if (w == 5 || w == 6) {
#pragma unroll
                    for (int q2 = 0; q2 < 2; ++q2) {
                        const int kb = (w - 5) * 128 + q2 * 64 + lane;
                        const bool last = kb == 255 && (b & 7) == 7 && (b >> 3) == 31;
                        const u32x3 r0 = fw[4 * q2], r1 = fw[4 * q2 + 1], i0 = fw[4 * q2 + 2], i1 = fw[4 * q2 + 3];
                        const unsigned fa = lbF + kb * 48;
                        *(LAS u32x4*)fa = u32x4{r0[0], r0[1], r0[2], last ? r1[1] : r1[0]};
                        *(LAS u32x4*)(fa + 16) = u32x4{last ? r1[2] : r1[1], r1[2], i0[0], i0[1]};
                        *(LAS u32x4*)(fa + 32) = u32x4{i0[2], last ? i1[1] : i1[0], last ? i1[2] : i1[1], i1[2]};
                    }
                }
                asm volatile("s_waitcnt vmcnt(0)" ::: "memory");
            } else if (seg == 1) {
                if (tid == 448) FSET(FL_CTX + (h * 8 + j) * 128);
                issueB(0); issueB(1);
            } else poll();
        };
        b2_run(b2in, b, Tr, lbA, lbB, lbF, hook);
        if (w == 7) {
            for (unsigned spin = 0; spin < SPIN_MAX && !got; ++spin) { __builtin_amdgcn_s_sleep(1); poll(); }
            __builtin_amdgcn_fence(__ATOMIC_ACQUIRE, "wavefront");
        }
        LBAR();
    }
    issueB(2); issueA(0, af[0]); issueA(1, af[1]); issueA(2, af[2]);
    f32x4 acc[2][9];
#pragma unroll
    for (int v = 0; v < 2; ++v)
#pragma unroll
        for (int n = 0; n < 9; ++n) acc[v][n] = f32x4{};
#define OWAIT(N, A) asm volatile("s_waitcnt vmcnt(" #N ") lgkmcnt(0)\n\ts_barrier" : "+v"(A[0]), "+v"(A[1]), "+v"(A[2]), "+v"(A[3]) :: "memory")
#pragma unroll
    for (int sg = 0; sg < 8; ++sg) {
        if (sg == 0) OWAIT(8, af[0]); else if (sg == 1) OWAIT(12, af[1]); else if (sg < 6) OWAIT(16, af[sg & 3]); else if (sg == 6) OWAIT(8, af[sg & 3]); else OWAIT(0, af[sg & 3]);
        const unsigned sb = lds0 + (sg & 3) * 36864 + kh * 18432 + l16;
        bf16x8 bf[18];
#pragma unroll
        for (int i = 0; i < 4; ++i) bf[i] = lds_rd128(sb + i * 1024);
        if (sg == 0 && tid == 0) FSET(FL_U + b * 8);
        if (sg + 3 < 8) { issueA(sg + 3, af[(sg + 3) & 3]); issueB(sg + 3); }
        if (sg == 7 && w == 7) {
#pragma unroll
            for (int i = 0; i < 4; ++i) pv_[i] = FGET(FL_U + (i * 64 + lane) * 8);
        }
        SB();
#pragma unroll
        for (int i = 0; i < 18; ++i) {
            if (i + 4 < 18) bf[i + 4] = lds_rd128(sb + (i + 4) * 1024);
            const int u = i / 9, n = i % 9;
            acc[0][n] = MFMA16(bf[i], af[sg & 3][u], acc[0][n]);
            acc[1][n] = MFMA16(bf[i], af[sg & 3][2 + u], acc[1][n]);
            SB();
        }
    }
#undef OWAIT
    if (w == 7) { wait_all(FL_U); __builtin_amdgcn_fence(__ATOMIC_ACQUIRE, "wavefront"); }
    LBAR();
    B3In b3in; b3_load(b3in, b, Tr);
    const unsigned rbx = lds0 + mi * 18432 + l16;
#pragma unroll
    for (int n = 0; n < 9; ++n) *(LAS f32x4*)(rbx + ((1 - kh) * 9 + n) * 1024) = kh ? acc[0][n] : acc[1][n];
    LBAR();
    const __amdgpu_buffer_rsrc_t xr = __builtin_amdgcn_make_buffer_rsrc((void*)(ws + WS_XCH + (size_t)idx * 16 * 18432), 0, 16 * 18432, 0x00020000);
#pragma unroll
    for (int n = 0; n < 9; ++n) {
        const f32x4 sum = (kh ? acc[1][n] : acc[0][n]) + *(const LAS f32x4*)(rbx + (kh * 9 + n) * 1024);
        if (mi == ks) *(LAS f32x4*)(rbx + (kh * 9 + n) * 1024) = sum;
        else __builtin_amdgcn_raw_buffer_store_b128(__builtin_bit_cast(u32x4, sum), xr, l16 + n * 1024, (mi * 4 + ks) * 18432 + kh * 9216, 16);
    }
    b3_part1(b3in, b, lds0 + 81920);
    WAIT_BAR0();
    if (tid == 0) FSET(FL_X + (idx * 4 + ks) * 128);
    if (b == 0 && tid == 448) FSET(FL_E);
    b3_part2(b3in, b, out, lds0 + 81920);
    if (w == 7) {
        for (unsigned spin = 0; spin < SPIN_MAX; ++spin) {
            const u64 v = FGET(FL_X + (idx * 4 + (lane & 3)) * 128);
            if (__all(v == ep)) break;
            __builtin_amdgcn_s_sleep(1);
        }
        __builtin_amdgcn_fence(__ATOMIC_ACQUIRE, "wavefront");
    }
    LBAR();
    for (int t = w; t < 18; t += 8) {
        f32x4 sum = *(const LAS f32x4*)(lds0 + ks * 18432 + t * 1024 + l16);
#pragma unroll
        for (int o = 1; o < 4; ++o)
            sum += __builtin_bit_cast(f32x4, __builtin_amdgcn_raw_buffer_load_b128(xr, l16 + t * 1024, (ks * 4 + ((ks + o) & 3)) * 18432, 16));
        const int v = t / 9, n = t - v * 9;
        u32x2 o2 = {cvtpk(sum[0], sum[1]), cvtpk(sum[2], sum[3])};
        *(u32x2*)(out + ((size_t)(mbk * 128 + ks * 32 + v * 16 + (lane & 15)) * OUTW + (nb * 9 + n) * 16 + 4 * (lane >> 4)) * 2) = o2;
    }
}

extern "C" void kernel_launch(void* const* d_in, const int* in_sizes, int n_in, void* d_out, int out_size, void* d_ws,
                              size_t ws_size, hipStream_t stream) {
    (void)in_sizes; (void)n_in; (void)out_size; (void)ws_size;
    const char* x = (const char*)d_in[0]; const char* c = (const char*)d_in[1]; const char* wq = (const char*)d_in[2];
    const char* wuk = (const char*)d_in[3]; const char* wuv = (const char*)d_in[4]; const char* wo = (const char*)d_in[5];
    const char* xb = (const char*)d_in[6];
    const unsigned short* wr = (const unsigned short*)d_in[7]; const unsigned short* wi = (const unsigned short*)d_in[8];
    char* ws = (char*)d_ws; char* out = (char*)d_out;
    (void)hipFuncSetAttribute((const void*)k_main, hipFuncAttributeMaxDynamicSharedMemorySize, 163840);
    k_main<<<256, 512, 163840, stream>>>(x, wq, c, wuk, wuv, ws, (const char*)wr, (const char*)wi, wo, xb, out);
}
```
